```python
import torch

def _softmax(x, axis=-1):
    return torch.softmax(x, dim=axis)
def _gelu(x):
    return torch.nn.functional.gelu(x, approximate="tanh")
def _silu(x):
    return torch.nn.functional.silu(x)
def _sigmoid(x):
    return torch.sigmoid(x)
def _layernorm(x, axis=-1, eps=1e-5):
    m = x.mean(dim=axis, keepdim=True)
    v = x.var(dim=axis, keepdim=True, unbiased=False)
    return (x - m) / torch.sqrt(v + eps)
def _scatter_add(tgt, idx, src, axis=0):
    return tgt.clone().index_add_(axis, idx, src)
def _segment_sum(src, seg, n):
    out = torch.zeros((n,) + src.shape[1:], dtype=src.dtype)
    return out.index_add_(0, seg, src)
def _pad(x, pw):
    flat = tuple(v for pair in reversed(pw) for v in pair)
    return torch.nn.functional.pad(x, flat)

kernel_name = 'gather_scatter__edge_featured_mpnn_egc__graph'

def setup_inputs():

    _w = (lambda a: (lambda t: t.to(torch.bfloat16) if t.is_floating_point() else t)(torch.as_tensor(a)))
    N, deg, D, De = 10000, 16, 256, max(4, 1)
    E = N * deg
    return {
        "x": _w((lambda *s: torch.randn(*s, dtype=torch.bfloat16) * 0.1)(N, D)),
        "e_attr": _w((lambda *s: torch.randn(*s, dtype=torch.bfloat16) * 0.1)(E, De)),
        "src": _w((lambda lo, hi, *s: torch.randint(lo, hi, s))(0, N, E).to(torch.int64)),
        "dst": _w((lambda lo, hi, *s: torch.randint(lo, hi, s))(0, N, E).to(torch.int64)),
        "u_w": _w((lambda *s: torch.randn(*s, dtype=torch.bfloat16) * 0.05)(D, D)),
        "v_w": _w((lambda *s: torch.randn(*s, dtype=torch.bfloat16) * 0.05)(D, D)),
        "g_w": _w((lambda *s: torch.randn(*s, dtype=torch.bfloat16) * 0.1)(2 * D + De, D)),
    }

def reference(x, e_attr, src, dst, u_w, v_w, g_w):
    N, D = x.shape
    g_in = torch.concatenate([x[dst], x[src], e_attr], axis=-1)
    eta = _sigmoid(g_in @ g_w)
    hv = x @ v_w
    msg = eta * hv[src]
    agg = _segment_sum(msg, dst, N)
    return _silu(x @ u_w + agg)
```

```cpp
#include <hip/hip_runtime.h>
#include <hip/hip_bf16.h>
#include <stdint.h>

typedef float    f32x4 __attribute__((ext_vector_type(4)));
typedef float    f32x2 __attribute__((ext_vector_type(2)));
typedef short    s16x4 __attribute__((ext_vector_type(4)));
typedef __bf16   bf16x8 __attribute__((ext_vector_type(8)));
typedef __bf16   bf16x2 __attribute__((ext_vector_type(2)));
typedef unsigned u32x4 __attribute__((ext_vector_type(4)));
struct u32x3 { unsigned x, y, z; };
typedef unsigned u32x2 __attribute__((ext_vector_type(2)));
#define LDSP __attribute__((address_space(3)))

#define STAMP(K, I) do {} while (0)

namespace {

constexpr int D     = 256;
constexpr int FS    = 64;
constexpr int NS    = 4;
constexpr int BM    = 160;
constexpr int NB    = 32;
constexpr int CAP   = 32;
constexpr int CMAX  = 64;
constexpr int LSTR  = 48;
constexpr int MAXD  = 40;
constexpr float NLOG2E = -1.4426950408889634f;
constexpr unsigned SENT = 0x7fffff00u;

__device__ __forceinline__ unsigned pack_bf16(float lo, float hi) {
    f32x2 p = {lo, hi};
    return __builtin_bit_cast(unsigned, __builtin_convertvector(p, bf16x2));
}
__device__ __forceinline__ float bf_lo(unsigned v) { return __builtin_bit_cast(float, v << 16); }
__device__ __forceinline__ float bf_hi(unsigned v) { return __builtin_bit_cast(float, v & 0xffff0000u); }

__device__ __forceinline__ unsigned pack_t1(float xb, float hv) {
    f32x2 p = {xb, xb};
    f32x2 sc = {NLOG2E, -NLOG2E};
    p = p * sc;
    unsigned P = __builtin_bit_cast(unsigned, __builtin_convertvector(p, bf16x2));
    unsigned t = __builtin_bit_cast(unsigned, hv) - (unsigned)((int)P >> 16);
    return (t & 0xffff0000u) | (P & 0xffffu);
}

constexpr int K1_XS = BM * 256;
constexpr int K1_WS = 128 * 512;
constexpr int K1_LDS = K1_XS + K1_WS + 4096;

__global__ __launch_bounds__(512, 2)
void egc_k1(const uint16_t* __restrict__ x, const uint16_t* __restrict__ ea,
            const int* __restrict__ src, const int* __restrict__ dst,
            const uint16_t* __restrict__ uw, const uint16_t* __restrict__ vw,
            const uint16_t* __restrict__ gw,
            unsigned* __restrict__ T1, unsigned* __restrict__ T2,
            unsigned* __restrict__ cells, unsigned char* __restrict__ counts,
            int N, int E, int nrb, int nbins, int ec)
{
    extern __shared__ __attribute__((aligned(16))) char smem[];
    char* xs = smem;
    char* ws = smem + K1_XS;
    unsigned* bcnt = (unsigned*)(smem + K1_XS + K1_WS);

    const int b   = blockIdx.x;
    const int rb  = (b & 7) + 8 * (b >> 5);
    const int s   = (b >> 3) & 3;
    if (rb >= nrb) return;
    const int tid = threadIdx.x;
    const int lane = tid & 63, w = tid >> 6;
    const int g = lane >> 4, n = lane & 15;
    const int row0 = rb * BM;
    const bool do_bin = (s == 0) && (rb < CMAX);

    STAMP(0, 0);
    u32x4 xr[2][5], wr[2][8];
#pragma unroll
    for (int c = 0; c < 2; ++c) {
#pragma unroll
        for (int i = 0; i < 8; ++i) {
            int p = tid + 512 * i;
            int k = p >> 5, m = (p >> 3) & 3, ch = p & 7;
            const uint16_t* base = (m == 0) ? gw + 256 * D : (m == 1) ? vw : (m == 2) ? gw : uw;
            wr[c][i] = *(const u32x4*)(base + (size_t)(c * 128 + k) * D + s * FS + ch * 8);
        }
#pragma unroll
        for (int i = 0; i < 5; ++i) {
            int p = tid + 512 * i;
            int r = p >> 4, ch = p & 15;
            int gr = min(row0 + r, N - 1);
            xr[c][i] = *(const u32x4*)(x + (size_t)gr * D + c * 128 + ch * 8);
        }
    }

    if (do_bin) {
        for (int i = tid; i < nbins; i += 512) bcnt[i] = 0;
        __syncthreads();
        const int e0 = rb * ec, e1 = min(E, e0 + ec);
        for (int eb = e0; eb < e1; eb += 512 * 5) {
            int dv[5], sv[5]; u32x2 ev[5];
#pragma unroll
            for (int i = 0; i < 5; ++i) {
                int e = min(eb + tid + 512 * i, e1 - 1);
                dv[i] = dst[e]; sv[i] = src[e];
                ev[i] = *(const u32x2*)(ea + (size_t)e * 4);
            }
#pragma unroll
            for (int i = 0; i < 5; ++i) {
                if (eb + tid + 512 * i < e1) {
                    int bin = dv[i] >> 5;
                    unsigned r = atomicAdd(&bcnt[bin], 1u);
                    if (r < CAP) {
                        u32x3 rec = {(unsigned)sv[i] * 32u + (unsigned)(dv[i] & 31), ev[i].x, ev[i].y};
                        *(u32x3*)(cells + ((size_t)(bin * CMAX + rb) * CAP + r) * 3) = rec;
                    }
                }
            }
        }
        __syncthreads();
        for (int i = tid; i < nbins; i += 512) counts[i * CMAX + rb] = (unsigned char)min(bcnt[i], 255u);
    }

    STAMP(0, 1);
    const int h = w & 1, u = w >> 1;
    f32x4 acc[4][5];
#pragma unroll
    for (int m = 0; m < 4; ++m)
#pragma unroll
        for (int t = 0; t < 5; ++t) {
            float z = (m == 1) ? 1e-36f : 0.f;
            acc[m][t] = f32x4{z, z, z, z};
        }

#pragma unroll
    for (int c = 0; c < 2; ++c) {
        if (c) __syncthreads();
#pragma unroll
        for (int i = 0; i < 8; ++i) {
            int p = tid + 512 * i;
            int k = p >> 5, un = (p >> 1) & 15, hf = p & 1;
            int swz = (k & 3) | (((k >> 3) & 1) << 2);
            *(u32x4*)(ws + k * 512 + ((un ^ swz) << 5) + hf * 16) = wr[c][i];
        }
#pragma unroll
        for (int i = 0; i < 5; ++i) {
            int p = tid + 512 * i;
            int r = p >> 4, ch = p & 15;
            *(u32x4*)(xs + r * 256 + ((ch ^ (r & 15)) << 4)) = xr[c][i];
        }
        __syncthreads();
        STAMP(0, 2 + c);
#pragma unroll
        for (int ks = 0; ks < 4; ++ks) {
            bf16x8 af[4], bfr[5];
            const int q = n >> 2, pp = n & 3;
#pragma unroll
            for (int m = 0; m < 4; ++m) {
                s16x4 lo, hi;
                {
                    int k = 32 * ks + 8 * g + q;
                    int swz = q | ((g & 1) << 2);
                    lo = __builtin_amdgcn_ds_read_tr16_b64_v4i16((LDSP s16x4*)(ws + k * 512 + (((m * 4 + u) ^ swz) << 5) + 8 * pp));
                    hi = __builtin_amdgcn_ds_read_tr16_b64_v4i16((LDSP s16x4*)(ws + (k + 4) * 512 + (((m * 4 + u) ^ swz) << 5) + 8 * pp));
                }
                u32x2 l2 = __builtin_bit_cast(u32x2, lo), h2 = __builtin_bit_cast(u32x2, hi);
                u32x4 a4 = {l2.x, l2.y, h2.x, h2.y};
                af[m] = __builtin_bit_cast(bf16x8, a4);
            }
#pragma unroll
            for (int t = 0; t < 5; ++t) {
                int r = h * 80 + t * 16 + n;
                bfr[t] = *(const bf16x8*)(xs + r * 256 + (((4 * ks + g) ^ n) << 4));
            }
#pragma unroll
            for (int m = 0; m < 4; ++m)
#pragma unroll
                for (int t = 0; t < 5; ++t)
                    acc[m][t] = __builtin_amdgcn_mfma_f32_16x16x32_bf16(af[m], bfr[t], acc[m][t], 0, 0, 0);
        }
    }

    STAMP(0, 4);
#pragma unroll
    for (int t = 0; t < 5; ++t) {
        int node = row0 + h * 80 + t * 16 + n;
        if (node < N) {
            u32x4 o1, o2;
#pragma unroll
            for (int r = 0; r < 4; ++r) {
                o1[r] = pack_t1(acc[0][t][r], acc[1][t][r]);
                o2[r] = pack_bf16(acc[2][t][r] * NLOG2E, acc[3][t][r]);
            }
            size_t o = ((size_t)s * N + node) * FS + u * 16 + g * 4;
            *(u32x4*)(T1 + o) = o1;
            *(u32x4*)(T2 + o) = o2;
        }
    }
    STAMP(0, 5);
    STAMP(0, 6);
}

constexpr int K2_OFFS = 0;
constexpr int K2_EV   = K2_OFFS + NB * LSTR * 4;
constexpr int K2_T2   = K2_EV + (NB + 1) * LSTR * 8;
constexpr int K2_CNT  = K2_T2 + NB * FS * 4;
constexpr int K2_RANK = K2_CNT + NB * 4;
constexpr int K2_ORD  = K2_RANK + NB * 4;
constexpr int K2_FLAG = K2_ORD + NB * 4;
constexpr int K2_LDS  = K2_FLAG + 16;

__device__ __forceinline__ u32x2 silu_pack(f32x4 y) {
    f32x4 o;
#pragma unroll
    for (int r = 0; r < 4; ++r) {
        float t = __builtin_amdgcn_exp2f(y[r] * NLOG2E);
        o[r] = y[r] * __builtin_amdgcn_rcpf(1.0f + t);
    }
    return u32x2{pack_bf16(o[0], o[1]), pack_bf16(o[2], o[3])};
}

__global__ __launch_bounds__(256, 5)
void egc_k2(const unsigned* __restrict__ T1, const unsigned* __restrict__ T2,
            const unsigned* __restrict__ cells, const unsigned char* __restrict__ counts,
            const uint16_t* __restrict__ gw, const uint16_t* __restrict__ ea,
            const int* __restrict__ src, const int* __restrict__ dst,
            uint16_t* __restrict__ out, int N, int E, int nchunks, int nbins, int force_slow)
{
    __shared__ __attribute__((aligned(16))) char smem[K2_LDS];
    unsigned* offs = (unsigned*)(smem + K2_OFFS);
    u32x2*    evl  = (u32x2*)(smem + K2_EV);
    unsigned* t2s  = (unsigned*)(smem + K2_T2);
    unsigned* cnt  = (unsigned*)(smem + K2_CNT);
    unsigned* rnk  = (unsigned*)(smem + K2_RANK);
    unsigned* ord  = (unsigned*)(smem + K2_ORD);
    unsigned* flag = (unsigned*)(smem + K2_FLAG);

    const int b   = blockIdx.x;
    const int s   = (b & 7) >> 1;
    const int bin = 2 * (b >> 3) + (b & 1);
    if (bin >= nbins) return;
    const int tid = threadIdx.x;
    const int lane = tid & 63, w = tid >> 6;
    const int g = lane >> 4, n = lane & 15;
    const int node0 = bin * NB;

    STAMP(1, 0);
    const int c = tid >> 2, j = tid & 3;
    unsigned nc = (c < nchunks) ? counts[bin * CMAX + c] : 0u;
    u32x4 t2r[2];
#pragma unroll
    for (int i = 0; i < 2; ++i) {
        int p = tid + 256 * i;
        int r = min(node0 + (p >> 4), N - 1);
        t2r[i] = *(const u32x4*)(T2 + ((size_t)s * N + r) * FS + (p & 15) * 4);
    }
    bf16x8 cfa, cfb;
    {
        const uint16_t* cw = gw + (size_t)2 * D * D + s * FS + 4 * n + g;
        float c0 = bf_lo(cw[0]) * NLOG2E, c1 = bf_lo(cw[D]) * NLOG2E;
        float c2 = bf_lo(cw[2 * D]) * NLOG2E, c3 = bf_lo(cw[3 * D]) * NLOG2E;
        u32x4 ca = {pack_bf16(c0, c1), pack_bf16(c2, c3), 0u, 0u};
        u32x4 cb = {0u, 0u, ca[0], ca[1]};
        cfa = __builtin_bit_cast(bf16x8, ca);
        cfb = __builtin_bit_cast(bf16x8, cb);
    }
    {
        u32x4 sv = {SENT, SENT, SENT, SENT}, zv = {0u, 0u, 0u, 0u};
        for (int i = tid; i < NB * LSTR / 4; i += 256) ((u32x4*)offs)[i] = sv;
        for (int i = tid; i < (NB + 1) * LSTR / 2; i += 256) ((u32x4*)evl)[i] = zv;
        if (tid < 25) ((u32x4*)cnt)[tid] = zv;
    }
    __syncthreads();

    STAMP(1, 1);
    bool bad = (nc > CAP) || force_slow;
    nc = min(nc, (unsigned)CAP);
    {
        const unsigned* cell = cells + (size_t)(bin * CMAX + c) * CAP * 3;
        for (int i0 = 0; i0 < CAP / 4; i0 += 4) {
            if (!__any(j + 4 * i0 < nc)) break;
            u32x3 rec[4];
#pragma unroll
            for (int i = 0; i < 4; ++i) {
                unsigned sl = j + 4 * (i0 + i);
                if (sl < nc) rec[i] = *(const u32x3*)(cell + sl * 3);
            }
#pragma unroll
            for (int i = 0; i < 4; ++i) {
                unsigned sl = j + 4 * (i0 + i);
                if (sl < nc) {
                    unsigned nl = rec[i].x & 31u;
                    unsigned r = atomicAdd(&cnt[nl], 1u);
                    if (r < MAXD) {
                        offs[nl * LSTR + r] = (rec[i].x >> 5) * (FS * 4);
                        evl[nl * LSTR + r] = u32x2{rec[i].y, rec[i].z};
                    } else bad = true;
                }
            }
        }
    }
    if (bad) flag[0] = 1u;
#pragma unroll
    for (int i = 0; i < 2; ++i) ((u32x4*)t2s)[tid + 256 * i] = t2r[i];
    __syncthreads();

    STAMP(1, 2);
    if (flag[0]) {
        float* accs = (float*)(smem + K2_EV);
        unsigned* queue = offs;
        __syncthreads();
        for (int i = tid; i < NB * FS; i += 256) accs[i] = 0.f;
        const int f = tid & 63;
        const uint16_t* cw = gw + (size_t)2 * D * D + s * FS + f;
        const float c0 = bf_lo(cw[0]) * NLOG2E, c1 = bf_lo(cw[D]) * NLOG2E;
        const float c2 = bf_lo(cw[2 * D]) * NLOG2E, c3 = bf_lo(cw[3 * D]) * NLOG2E;
        for (int eb = 0; eb < E; eb += 256) {
            if (tid == 0) flag[1] = 0u;
            __syncthreads();
            int e = eb + tid;
            if (e < E) {
                int dn = dst[e] - node0;
                if (dn >= 0 && dn < NB) queue[atomicAdd(&flag[1], 1u)] = e;
            }
            __syncthreads();
            int nq = flag[1];
            for (int qi = w; qi < nq; qi += 4) {
                int ee = queue[qi];
                int dn = dst[ee] - node0;
                unsigned tv = T1[((size_t)s * N + src[ee]) * FS + f];
                u32x2 e2 = *(const u32x2*)(ea + (size_t)ee * 4);
                float z = bf_lo(t2s[dn * FS + f]) + bf_lo(tv) + bf_lo(e2.x) * c0 + bf_hi(e2.x) * c1
                          + bf_lo(e2.y) * c2 + bf_hi(e2.y) * c3;
                float m = __builtin_bit_cast(float, tv) * __builtin_amdgcn_rcpf(1.0f + __builtin_amdgcn_exp2f(z));
                atomicAdd(&accs[dn * FS + f], m);
            }
            __syncthreads();
        }
        for (int p = tid; p < NB * 16; p += 256) {
            int r = p >> 4, q4 = (p & 15) * 4;
            if (node0 + r < N) {
                f32x4 y;
#pragma unroll
                for (int k = 0; k < 4; ++k) y[k] = accs[r * FS + q4 + k] + bf_hi(t2s[r * FS + q4 + k]);
                *(u32x2*)(out + (size_t)(node0 + r) * D + s * FS + q4) = silu_pack(y);
            }
        }
        return;
    }

    {
        int i = tid & 31, p = tid >> 5;
        unsigned ki = cnt[i] * 32u + i;
        u32x4 c4 = ((const u32x4*)cnt)[p];
        unsigned r = 0;
#pragma unroll
        for (int k = 0; k < 4; ++k) r += (c4[k] * 32u + (4 * p + k) > ki) ? 1u : 0u;
        atomicAdd(&rnk[i], r);
        __syncthreads();
        if (tid < 32) ord[rnk[tid]] = ki;
        __syncthreads();
    }

    STAMP(1, 3);
    bf16x8 asel;
    {
        u32x4 a4;
#pragma unroll
        for (int k = 0; k < 4; ++k) a4[k] = (g == (n >> 2) && k == (n & 3)) ? 0x3f80u : 0u;
        asel = __builtin_bit_cast(bf16x8, a4);
    }
    const __amdgpu_buffer_rsrc_t t1r =
        __builtin_amdgcn_make_buffer_rsrc((void*)(T1 + (size_t)s * N * FS), 0, N * FS * 4, 0x00020000);
    const unsigned lo16 = n * 16;

#pragma unroll 1
    for (int qq = 0; qq < 2; ++qq) {
        const int quad = qq ? 7 - w : w;
        const unsigned kg = ord[4 * quad + g];
        const int nl = kg & 31;
        const int steps = __builtin_amdgcn_readfirstlane(kg >> 5);
        const int enl = ((n & 3) == g) ? (int)(ord[4 * quad + (n >> 2)] & 31) : NB;
        const u32x4* op = (const u32x4*)(offs + nl * LSTR);
        const u32x4* ep = (const u32x4*)(evl + enl * LSTR);
        const u32x4 t2 = *(const u32x4*)(t2s + nl * FS + n * 4);
        f32x4 cin = {bf_lo(t2[0]), bf_lo(t2[1]), bf_lo(t2[2]), bf_lo(t2[3])};
        f32x4 acc = {bf_hi(t2[0]), bf_hi(t2[1]), bf_hi(t2[2]), bf_hi(t2[3])};

        u32x4 dcur[4], dnxt[4];
        {
            u32x4 o4 = op[0];
#pragma unroll
            for (int k = 0; k < 4; ++k) dcur[k] = __builtin_amdgcn_raw_buffer_load_b128(t1r, o4[k] + lo16, 0, 0);
        }
#pragma unroll 1
        for (int st = 0; st < steps; st += 4) {
            u32x4 o4 = op[(st >> 2) + 1];
            u32x4 e01 = ep[(st >> 1)], e23 = ep[(st >> 1) + 1];
#pragma unroll
            for (int k = 0; k < 4; ++k) dnxt[k] = __builtin_amdgcn_raw_buffer_load_b128(t1r, o4[k] + lo16, 0, 0);
#pragma unroll
            for (int k = 0; k < 4; ++k) {
                if (k == 0 || st + k < steps) {
                    f32x4 z = __builtin_amdgcn_mfma_f32_16x16x32_bf16(__builtin_bit_cast(bf16x8, k < 2 ? e01 : e23), (k & 1) ? cfb : cfa, cin, 0, 0, 0);
                    z = __builtin_amdgcn_mfma_f32_16x16x32_bf16(asel, __builtin_bit_cast(bf16x8, dcur[k]), z, 0, 0, 0);
                    f32x4 hv = __builtin_bit_cast(f32x4, dcur[k]);
                    f32x4 a;
#pragma unroll
                    for (int r = 0; r < 4; ++r) a[r] = __builtin_amdgcn_exp2f(z[r]);
                    a = a + 1.0f;
#pragma unroll
                    for (int r = 0; r < 4; ++r) a[r] = __builtin_amdgcn_rcpf(a[r]);
                    acc = __builtin_elementwise_fma(a, hv, acc);
                }
            }
#pragma unroll
            for (int k = 0; k < 4; ++k) dcur[k] = dnxt[k];
        }
        const int node = node0 + nl;
        if (node < N)
            *(u32x2*)(out + (size_t)node * D + s * FS + n * 4) = silu_pack(acc);
        STAMP(1, 4 + qq);
    }
}

}

extern "C" void kernel_launch(void* const* d_in, const int* in_sizes, int n_in,
                              void* d_out, int out_size, void* d_ws, size_t ws_size,
                              hipStream_t stream) {
    const uint16_t* x  = (const uint16_t*)d_in[0];
    const uint16_t* ea = (const uint16_t*)d_in[1];
    const int* src     = (const int*)d_in[2];
    const int* dst     = (const int*)d_in[3];
    const uint16_t* uw = (const uint16_t*)d_in[4];
    const uint16_t* vw = (const uint16_t*)d_in[5];
    const uint16_t* gw = (const uint16_t*)d_in[6];
    const int N = in_sizes[0] / D;
    const int E = in_sizes[2];
    const int nrb   = (N + BM - 1) / BM;
    const int nbins = (N + NB - 1) / NB;
    const int nchunks = nrb < CMAX ? nrb : CMAX;
    const int ec = (E + nchunks - 1) / nchunks;
    const int force_slow = (nbins > 1024) ? 1 : 0;

    char* ws = (char*)d_ws;
    size_t tb = ((size_t)NS * N * FS * 4 + 255) & ~(size_t)255;
    unsigned* T1 = (unsigned*)ws;
    unsigned* T2 = (unsigned*)(ws + tb);
    unsigned* cells = (unsigned*)(ws + 2 * tb);
    unsigned char* counts = (unsigned char*)(ws + 2 * tb + (size_t)nbins * CMAX * CAP * 12);

    (void)hipFuncSetAttribute((const void*)egc_k1, hipFuncAttributeMaxDynamicSharedMemorySize, K1_LDS);
    int g1 = ((nrb + 7) / 8) * 32;
    egc_k1<<<g1, 512, K1_LDS, stream>>>(x, ea, src, dst, uw, vw, gw, T1, T2, cells, counts,
                                        N, force_slow ? 0 : E, nrb, force_slow ? 0 : nbins, ec);
    int g2 = ((nbins + 1) / 2) * 8;
    egc_k2<<<g2, 256, 0, stream>>>(T1, T2, cells, counts, gw, ea, src, dst, (uint16_t*)d_out,
                                   N, E, force_slow ? 0 : nchunks, nbins, force_slow);
}
```

```cpp
#include <hip/hip_runtime.h>
#include <hip/hip_bf16.h>
#include <stdint.h>

typedef float    f32x4 __attribute__((ext_vector_type(4)));
typedef float    f32x2 __attribute__((ext_vector_type(2)));
typedef short    s16x4 __attribute__((ext_vector_type(4)));
typedef __bf16   bf16x8 __attribute__((ext_vector_type(8)));
typedef __bf16   bf16x2 __attribute__((ext_vector_type(2)));
typedef unsigned u32x4 __attribute__((ext_vector_type(4)));
typedef unsigned u32x3 __attribute__((ext_vector_type(3)));
typedef unsigned u32x2 __attribute__((ext_vector_type(2)));
typedef __amdgpu_buffer_rsrc_t rsrc_t;
#define LDSP __attribute__((address_space(3)))

#define BLK_DECL
#define BLK_INIT
#define BLK_STAMP
#define BLK_LAST
#define BLK_OUT
#define BLK_FLUSH
#define STAMP_DECL
#define STAMP_ARG
#define STAMP_PASS
#define STAMP_FLUSH(K, L) do {} while (0)
#define STAMP(K, I) do {} while (0)
#define WSTAMP(I, V) do {} while (0)

#define AB_M(x) x
#define AB_G(x) x
#define AB_E(x) x
#define AB_T(x) x
#define NBK 4
#define EXP_PF 2
#define EXP_K1A 0
#define EXP_BSL 40
#define EXP_P1 20
#define EXP_P2 10
#define EXP_ROT 0
namespace {

constexpr int D     = 256;
constexpr int FS    = 64;
constexpr int NS    = 4;
constexpr int BM    = 384;
constexpr int NB    = 80;
constexpr int CMAX  = 42;
constexpr int TSTR  = 64;
constexpr int RMAX  = 2;
constexpr int BINMAX = 256;
constexpr int SLOTS = 5;
constexpr int TPC   = 12;
constexpr int LSTR  = 72;
constexpr int MAXT  = 59;
constexpr unsigned MINL = 8;
constexpr float NLOG2E = -1.4426950408889634f;
constexpr unsigned SENT = 0x00fff000u;
constexpr unsigned OOB  = 0x7ffffff0u;
constexpr int AUX_WT = 16;

__device__ __forceinline__ rsrc_t mk_rsrc(const void* p, unsigned bytes) {
    return __builtin_amdgcn_make_buffer_rsrc((void*)p, (short)0, (int)bytes, 0x00020000);
}
__device__ __forceinline__ void dma16(rsrc_t r, unsigned voff, unsigned soff, unsigned lds) {
    asm volatile("s_mov_b32 m0, %0\n\ts_nop 0\n\tbuffer_load_dwordx4 %1, %2, %3 offen lds"
                 :: "s"(lds), "v"(voff), "s"(r), "s"(soff) : "m0", "memory");
}
#define WAIT_V(n) asm volatile("s_waitcnt vmcnt(%0)" :: "i"(n) : "memory")
#define WAIT_L0() asm volatile("s_waitcnt lgkmcnt(0)" ::: "memory")
#define BAR() __builtin_amdgcn_s_barrier()

__device__ __forceinline__ unsigned wave_scan(unsigned v) {
    v += __builtin_amdgcn_update_dpp(0u, v, 0x111, 0xf, 0xf, false);
    v += __builtin_amdgcn_update_dpp(0u, v, 0x112, 0xf, 0xf, false);
    v += __builtin_amdgcn_update_dpp(0u, v, 0x114, 0xf, 0xf, false);
    v += __builtin_amdgcn_update_dpp(0u, v, 0x118, 0xf, 0xf, false);
    v += __builtin_amdgcn_update_dpp(0u, v, 0x142, 0xa, 0xf, false);
    v += __builtin_amdgcn_update_dpp(0u, v, 0x143, 0xc, 0xf, false);
    return v;
}
__device__ __forceinline__ unsigned pack_bf16(float lo, float hi) {
    f32x2 p = {lo, hi};
    return __builtin_bit_cast(unsigned, __builtin_convertvector(p, bf16x2));
}
__device__ __forceinline__ float bf_lo(unsigned v) { return __builtin_bit_cast(float, v << 16); }
__device__ __forceinline__ float bf_hi(unsigned v) { return __builtin_bit_cast(float, v & 0xffff0000u); }

__device__ __forceinline__ unsigned pack_t1(float xb, float hv) {
    const float p = xb * NLOG2E;
    unsigned P;
    asm("v_cvt_pk_bf16_f32 %0, %1, -%1" : "=v"(P) : "v"(p));
    const unsigned t = __builtin_bit_cast(unsigned, hv) - (unsigned)((int)P >> 16);
    return (t & 0xffff0000u) | (P & 0xffffu);
}

constexpr int K1_W   = 98304;
constexpr int K1_LDS = 163840;
__host__ __device__ constexpr int K1_R(int r) { return r == 5 ? 0 : r * 32768; }

template <int T>
__device__ __forceinline__ unsigned k1_pack1(float a, float b) {
    asm("" : "+v"(a));
    return T ? pack_bf16(a * NLOG2E, b) : pack_t1(a, b);
}

template <int T>
__device__ __forceinline__ void k1_gemm(char* smem, const unsigned lds0, const int lane, const int w, const int s, const int rot,
                                        const rsrc_t xr, const rsrc_t wr, const rsrc_t tr STAMP_ARG) {
    const int g = lane >> 4, n = lane & 15;
    const int fh = w & 1, rg = w >> 1, u = w & 3;
    const unsigned rl = 2 * w + (lane >> 5);
    const unsigned xvo = rl * 512 + (((lane & 31) ^ rl) << 4);
    const unsigned f = ((lane >> 4) & 1) | ((u & 1) << 1);
    const unsigned wvo = (lane >> 3) * 512 + s * 128 + ((((lane >> 1) & 3) ^ f) << 5) + (lane & 1) * 16 + u * 4096;
    const unsigned ldw = lds0 + w * 1024;
    unsigned gr[6];
    gr[0] = 0; gr[1] = 1;
#pragma unroll
    for (int r = 2; r < 6; ++r) gr[r] = 2 + ((rot + r) & 3);
#define K1_LDW(ks)   dma16(wr, wvo, (ks) * 16384, ldw + K1_W + (ks) * 8192)
#define K1_LDX(r, i) dma16(xr, xvo, gr[r] * 32768 + (i) * 8192, ldw + K1_R(r) + (i) * 8192)
#pragma unroll
    for (int ks = 0; ks < 8; ++ks) K1_LDW(ks);
#pragma unroll
    for (int i = 0; i < 4; ++i) K1_LDX(0, i);
    STAMP(0, 1);

    const float z1 = T ? 0.f : 1e-36f;
    const f32x4 zero4 = {0.f, 0.f, 0.f, 0.f}, tiny4 = {z1, z1, z1, z1};
    bf16x8 wf[8][4];
    bf16x8 xq[8];
    f32x4 acc[2][4];
    const unsigned fr = ((n >> 3) & 1) | ((g & 1) << 1);
    const char* wp0 = smem + K1_W + (8 * g + (n >> 2)) * 128 + (((2 * fh) ^ fr) << 5) + 8 * (n & 3);
    const char* wp1 = smem + K1_W + (8 * g + (n >> 2)) * 128 + (((2 * fh + 1) ^ fr) << 5) + 8 * (n & 3);
    unsigned xa[3][4];
#pragma unroll
    for (int k = 0; k < 4; ++k) {
        xa[0][k] = lds0 + n * 512 + rg * 8192 + (((4 * k + g) ^ n) << 4);
        xa[1][k] = xa[0][k] + 65536; xa[2][k] = xa[0][k] + 131072;
    }
    const unsigned ovo = g * 1024 + fh * 128 + n * 8 + rg * 4096;
#define K1_RDW(ks) do { _Pragma("unroll") for (int mf = 0; mf < 4; ++mf) {                                   \
        const char* wp = ((mf & 1) ? wp1 : wp0) + (ks) * 8192 + (mf >> 1) * 4096;                           \
        u32x2 l2 = __builtin_bit_cast(u32x2, __builtin_amdgcn_ds_read_tr16_b64_v4i16((LDSP s16x4*)wp));      \
        u32x2 h2 = __builtin_bit_cast(u32x2, __builtin_amdgcn_ds_read_tr16_b64_v4i16((LDSP s16x4*)(wp + 512))); \
        wf[ks][mf] = __builtin_bit_cast(bf16x8, u32x4{l2.x, l2.y, h2.x, h2.y}); } } while (0)
#define K1_RDX(r, ks) xq[ks] = *(const LDSP bf16x8*)(size_t)(xa[K1_R(r) >> 16][(ks) & 3] + (K1_R(r) & 65535) + ((ks) >> 2) * 256)
#define K1_MM(p, ks) do { _Pragma("unroll") for (int m = 0; m < 4; ++m)                                      \
        acc[p][m] = __builtin_amdgcn_mfma_f32_16x16x32_bf16(xq[ks], wf[ks][m], (ks) ? acc[p][m] : (m < 2 ? zero4 : tiny4), 0, 0, 0); } while (0)
#define SB() __builtin_amdgcn_sched_barrier(0)

    WAIT_V(4); BAR();
    STAMP(0, 2);
#pragma unroll
    for (int ks = 0; ks < 4; ++ks) { K1_RDW(ks); K1_LDX(1, ks); }
    SB();
    STAMP(0, 14);
    WAIT_V(4); BAR();
    STAMP(0, 15);
#pragma unroll
    for (int ks = 0; ks < 8; ++ks) K1_RDX(0, ks);
    SB();
#pragma unroll
    for (int ks = 0; ks < 4; ++ks) { K1_RDW(4 + ks); K1_MM(0, ks); K1_LDX(2, ks); SB(); }
    WAIT_L0(); WAIT_V(4); BAR();
    STAMP(0, 3);
#pragma unroll
    for (int ks = 0; ks < 4; ++ks) K1_RDX(1, ks);
    SB();
#pragma unroll
    for (int ks = 4; ks < 8; ++ks) { K1_MM(0, ks); K1_RDX(1, ks); K1_LDX(3, ks - 4); SB(); }

#pragma unroll
    for (int r = 1; r < 6; ++r) {
        const int p = r & 1, q = p ^ 1;
        if (r == 1) { WAIT_V(4); BAR(); STAMP(0, 4); }
        if (r == 2) { STAMP(0, 7); WAIT_V(12); BAR(); STAMP(0, 8); }
        if (r == 3) { WAIT_V(4); BAR(); STAMP(0, 9); }
        if (r == 4) STAMP(0, 11);
        if (r == 5) STAMP(0, 12);
        const unsigned vo = ovo + gr[r - 1] * 16384;
        u32x2 o;
#pragma unroll
        for (int ks = 0; ks < 8; ++ks) {
            K1_MM(p, ks);
            if (r < 5) K1_RDX(r + 1, ks);
            o[ks & 1] = k1_pack1<T>(acc[q][ks & 1][ks >> 1], acc[q][2 + (ks & 1)][ks >> 1]);
            if (ks & 1) __builtin_amdgcn_raw_buffer_store_b64(o, tr, vo + (ks >> 1) * 256, 0, AUX_WT);
            if (r == 1) K1_LDX(4 + (ks >> 2), ks & 3);
#pragma unroll
            for (int i = 0; i < 4; ++i) {
                __builtin_amdgcn_sched_group_barrier(0x008, 1, 0);
                __builtin_amdgcn_sched_group_barrier(0x002, 1, 0);
            }
            SB();
        }
    }
    STAMP(0, 13);
#pragma unroll
    for (int r = 0; r < 4; ++r) {
        const u32x2 o = {k1_pack1<T>(acc[1][0][r], acc[1][2][r]), k1_pack1<T>(acc[1][1][r], acc[1][3][r])};
        __builtin_amdgcn_raw_buffer_store_b64(o, tr, ovo + gr[5] * 16384 + r * 256, 0, AUX_WT);
    }
#undef K1_LDW
#undef K1_LDX
}

__global__ __launch_bounds__(512, 2)
void egc_k1(const uint16_t* __restrict__ x, const uint16_t* __restrict__ ea,
            const int* __restrict__ src, const int* __restrict__ dst,
            const uint16_t* __restrict__ uw, const uint16_t* __restrict__ vw,
            const uint16_t* __restrict__ gw,
            unsigned* __restrict__ T1, unsigned* __restrict__ T2,
            unsigned* __restrict__ recs, unsigned* __restrict__ tab,
            int N, int E, int nrb, int nbins, int ec, int nchunks, int rows, int bmul)
{
    extern __shared__ __attribute__((aligned(16))) char smem[];
    const int b   = blockIdx.x;
    const int slot = b >> 3;
    const int item = (b & 7) * nrb + slot;
    const int rb  = item >> 3;
    const int ct  = item & 7;
    const int tid = threadIdx.x;
    const int lane = tid & 63;
    const int w = __builtin_amdgcn_readfirstlane(tid >> 6);
    asm volatile("" :: "s"(x), "s"(ea), "s"(src), "s"(dst), "s"(uw), "s"(vw), "s"(gw), "s"(T1), "s"(T2), "s"(recs), "s"(tab),
                       "s"(N), "s"(E), "s"(nrb), "s"(nbins), "s"(ec), "s"(nchunks), "s"(rows), "s"(bmul));
    STAMP_DECL;
    STAMP(0, 0);

    if (slot >= nrb) {
        const int c = (slot - nrb) * 8 + (b & 7);
        if (c >= nchunks) return;
        unsigned* cw  = (unsigned*)smem;
        unsigned* wt  = cw + 8 * BINMAX;
        unsigned* rec = wt + 16;
        const int e0 = c * ec;
        const int ne = max(min(E - e0, ec), 0);
        const rsrc_t dr = mk_rsrc(dst + e0, ne * 4), sr = mk_rsrc(src + e0, ne * 4), er = mk_rsrc(ea + (size_t)e0 * 4, ne * 8);
        u32x4 dv[RMAX], sv[RMAX], ev[RMAX][2];
#pragma unroll
        for (int r = 0; r < RMAX; ++r) dv[r] = __builtin_amdgcn_raw_buffer_load_b128(dr, tid * 16, r * 8192, 0);
        ((u32x4*)cw)[tid] = u32x4{0u, 0u, 0u, 0u};
        __builtin_amdgcn_sched_barrier(0);
        __builtin_amdgcn_s_sleep(EXP_BSL);
        __builtin_amdgcn_sched_barrier(0);
#pragma unroll
        for (int r = 0; r < RMAX; ++r) {
            sv[r] = __builtin_amdgcn_raw_buffer_load_b128(sr, tid * 16, r * 8192, 0);
            ev[r][0] = __builtin_amdgcn_raw_buffer_load_b128(er, tid * 32, r * 16384, 0);
            ev[r][1] = __builtin_amdgcn_raw_buffer_load_b128(er, tid * 32 + 16, r * 16384, 0);
        }
        __builtin_amdgcn_sched_barrier(0);
        __syncthreads();
        unsigned* cmine = cw + w * BINMAX;
        unsigned rk[RMAX][4], bn[RMAX][4];
#pragma unroll
        for (int r = 0; r < RMAX; ++r)
#pragma unroll
            for (int k = 0; k < 4; ++k) {
                bn[r][k] = __umul24(dv[r][k], 52429u) >> 22;
                rk[r][k] = 0;
                if (r * 2048 + tid * 4 + k < ne) rk[r][k] = atomicAdd(&cmine[bn[r][k]], 1u);
            }
        __syncthreads();
        STAMP(2, 1);
        {
            unsigned pre[8], t = 0, inc = 0;
            if (tid < BINMAX) {
#pragma unroll
                for (int k = 0; k < 8; ++k) { pre[k] = t; t += cw[k * BINMAX + tid]; }
                inc = wave_scan(t);
                if (lane == 63) wt[w] = inc;
            }
            __syncthreads();
            if (tid < BINMAX) {
                const u32x4 w4 = *(const u32x4*)wt;
                const unsigned st = inc - t + (w > 0 ? w4[0] : 0u) + (w > 1 ? w4[1] : 0u) + (w > 2 ? w4[2] : 0u);
#pragma unroll
                for (int k = 0; k < 8; ++k) cw[k * BINMAX + tid] = st + pre[k];
                if (tid < nbins) tab[tid * TSTR + c] = st | (t << 16);
            }
        }
        __syncthreads();
        STAMP(2, 2);
#pragma unroll
        for (int r = 0; r < RMAX; ++r)
#pragma unroll
            for (int k = 0; k < 4; ++k) rk[r][k] += cmine[bn[r][k]];
#pragma unroll
        for (int r = 0; r < RMAX; ++r)
#pragma unroll
            for (int k = 0; k < 4; ++k)
                if (r * 2048 + tid * 4 + k < ne) {
                    unsigned* p = rec + rk[r][k] * 3;
                    p[0] = ev[r][k >> 1][(k & 1) * 2];
                    p[1] = ev[r][k >> 1][(k & 1) * 2 + 1];
                    p[2] = (sv[r][k] << 16) + (dv[r][k] + ((__umul24(sv[r][k], bmul) >> 16) - bn[r][k]) * NB) * 16u;
                }
        __syncthreads();
        STAMP(2, 3);
        const rsrc_t orr = mk_rsrc(recs + (size_t)e0 * 3, ne * 12);
#pragma unroll
        for (int i = 0; i < RMAX * 3; ++i) {
            if (i * 8192 < ne * 12) {
                u32x4 v = ((const u32x4*)rec)[tid + 512 * i];
                __builtin_amdgcn_raw_buffer_store_b128(v, orr, tid * 16 + i * 8192, 0, AUX_WT);
            }
        }
        STAMP(2, 5);
        STAMP(2, 6);
        STAMP_FLUSH(2, 6);
        return;
    }

    const int s = ct >> 1;
    const int row0 = rb * rows;
    const unsigned nr = (unsigned)max(min(rows, N - row0), 0);
    const unsigned lds0 = (unsigned)(size_t)(LDSP char*)smem;
    const rsrc_t xr = mk_rsrc(x + (size_t)row0 * D, nr * 512u);
    const size_t to = ((size_t)s * N + row0) * FS;
    if (ct & 1)
        k1_gemm<1>(smem, lds0, lane, w, s, ct, xr, mk_rsrc(w < 4 ? gw : uw, D * D * 2), mk_rsrc(T2 + to, nr * 256u) STAMP_PASS);
    else
        k1_gemm<0>(smem, lds0, lane, w, s, ct, xr, mk_rsrc(w < 4 ? gw + D * D : vw, D * D * 2), mk_rsrc(T1 + to, nr * 256u) STAMP_PASS);
    STAMP(0, 5);
    STAMP(0, 6);
    STAMP_FLUSH(0, 6);
}

constexpr int K2_OFFS = 0;
constexpr int K2_EV   = K2_OFFS + 32 * LSTR * 4;
constexpr int K2_CNT  = K2_EV + 33 * LSTR * 8;
constexpr int K2_HIST = K2_CNT + NBK * NB * 16;
constexpr int K2_ORD  = K2_HIST + 256;
constexpr int K2_LEN  = K2_ORD + 128;
constexpr int K2_FLAG = K2_LEN + 32;
constexpr int K2_PF   = K2_FLAG + 16;
constexpr int K2_LDS  = K2_PF + 8192;
static_assert((K2_PF - K2_EV) % 16 == 0 && K2_EV % 16 == 0, "LDS map");

__device__ __forceinline__ u32x2 silu_pack(f32x4 y) {
    f32x4 o;
#pragma unroll
    for (int r = 0; r < 4; ++r) {
        float t = __builtin_amdgcn_exp2f(y[r] * NLOG2E);
        o[r] = y[r] * __builtin_amdgcn_rcpf(1.0f + t);
    }
    return u32x2{pack_bf16(o[0], o[1]), pack_bf16(o[2], o[3])};
}

__device__ __forceinline__ void k2_slow(char* smem, const unsigned* __restrict__ T1, const unsigned* __restrict__ T2,
                                        const uint16_t* __restrict__ gw, const uint16_t* __restrict__ ea,
                                        const int* __restrict__ src, const int* __restrict__ dst,
                                        uint16_t* __restrict__ out, int N, int E, int s, int node0) {
    float* accs = (float*)smem;
    unsigned* queue = (unsigned*)(smem + NB * FS * 4);
    static_assert(NB * FS * 4 + 2048 <= K2_CNT, "slow-path LDS");
    const unsigned* t2g = T2 + ((size_t)s * N + node0) * FS;
    const int tid = threadIdx.x, f = tid & 63, w = tid >> 6;
    __syncthreads();
    for (int i = tid; i < NB * FS; i += 512) accs[i] = 0.f;
    const uint16_t* cf = gw + (size_t)2 * D * D + s * FS + (f & 32) + (f & 1) * 16 + ((f & 31) >> 1);
    const float c0 = bf_lo(cf[0]) * NLOG2E, c1 = bf_lo(cf[D]) * NLOG2E;
    const float c2 = bf_lo(cf[2 * D]) * NLOG2E, c3 = bf_lo(cf[3 * D]) * NLOG2E;
#pragma unroll 1
    for (int eb = 0; eb < E; eb += 512) {
        __syncthreads();
        const int e = eb + tid;
        const int dn = (e < E) ? dst[e] - node0 : -1;
        queue[tid] = (dn >= 0 && dn < NB) ? (unsigned)dn : 0xffffffffu;
        __syncthreads();
#pragma unroll 1
        for (int q0 = 0; q0 < 512; q0 += 64) {
            const unsigned qv = queue[q0 + f];
            unsigned long long m = __ballot(qv != 0xffffffffu && (qv & 7u) == (unsigned)w);
            while (m) {
                const int l = __builtin_ctzll(m);
                m &= m - 1;
                const int ee = eb + q0 + l;
                const int d2 = __builtin_amdgcn_readlane(qv, l);
                const unsigned tv = T1[((size_t)s * N + src[ee]) * FS + f];
                const u32x2 e2 = *(const u32x2*)(ea + (size_t)ee * 4);
                const float z = bf_lo(t2g[d2 * FS + f]) + bf_lo(tv) + bf_lo(e2.x) * c0 + bf_hi(e2.x) * c1
                                + bf_lo(e2.y) * c2 + bf_hi(e2.y) * c3;
                accs[d2 * FS + f] += __builtin_bit_cast(float, tv) * __builtin_amdgcn_rcpf(1.0f + __builtin_amdgcn_exp2f(z));
            }
        }
    }
    __syncthreads();
#pragma unroll 1
    for (int p = tid; p < NB * 16; p += 512) {
        const int r = p >> 4, q4 = (p & 15) * 4;
        if (node0 + r < N) {
            f32x4 y;
#pragma unroll
            for (int k = 0; k < 4; ++k) {
                const int sl = r * FS + (q4 & 32) + ((q4 & 15) + k) * 2 + ((q4 >> 4) & 1);
                y[k] = accs[sl] + bf_hi(t2g[sl]);
            }
            *(u32x2*)(out + (size_t)(node0 + r) * D + s * FS + q4) = silu_pack(y);
        }
    }
}

__global__ __launch_bounds__(512, 4)
void egc_k2(const unsigned* __restrict__ T1, const unsigned* __restrict__ T2,
            const unsigned* __restrict__ recs, const unsigned* __restrict__ tab,
            const uint16_t* __restrict__ gw, const uint16_t* __restrict__ ea,
            const int* __restrict__ src, const int* __restrict__ dst,
            uint16_t* __restrict__ out, int N, int E, int nchunks, int nbins, int ec, int force_slow)
{
    __shared__ __attribute__((aligned(16))) char smem[K2_LDS];
    unsigned* hist = (unsigned*)(smem + K2_HIST);
    unsigned* flag = (unsigned*)(smem + K2_FLAG);

    asm volatile("" :: "s"(T1), "s"(T2), "s"(recs), "s"(tab), "s"(gw), "s"(out), "s"(N), "s"(E), "s"(nchunks),
                       "s"(nbins), "s"(ec), "s"(force_slow));
    const int b   = blockIdx.x;
    const int s   = (b & 7) >> 1;
    const int hb  = (nbins + 1) >> 1;
    const int bin = (b & 1) * hb + (b >> 3);
    const int tid = threadIdx.x;
    const int lane = tid & 63;
    const int w = __builtin_amdgcn_readfirstlane(tid >> 6);
    const int g = lane >> 4, n = lane & 15;
    const int node0 = bin * NB;
    const unsigned lds0 = (unsigned)(size_t)(LDSP char*)smem;
    const rsrc_t t1r = mk_rsrc(T1 + (size_t)s * N * FS, (unsigned)N * 256u);
    __builtin_amdgcn_s_setprio(3);
    STAMP_DECL;
    STAMP(1, 0);

    const unsigned c = __umul24(tid, 5462u) >> 16, j = tid - c * TPC;
    unsigned te, cw0, cw1, cw2, cw3;
    asm volatile("buffer_load_dword %0, %5, %7, 0 offen\n\t"
                 "buffer_load_ushort %1, %6, %8, 0 offen\n\t"
                 "buffer_load_ushort %2, %6, %8, 0 offen offset:512\n\t"
                 "buffer_load_ushort %3, %6, %8, 0 offen offset:1024\n\t"
                 "buffer_load_ushort %4, %6, %8, 0 offen offset:1536"
                 : "=&v"(te), "=&v"(cw0), "=&v"(cw1), "=&v"(cw2), "=&v"(cw3)
                 : "v"(c * 4), "v"((n >> 3) * 64 + (n & 7) * 4 + (g >> 1) * 2 + (g & 1) * 32), "s"(mk_rsrc(tab + bin * TSTR, bin < nbins ? nchunks * 4 : 0)),
                   "s"(mk_rsrc(gw + (size_t)2 * D * D + s * FS, 2048 - s * 128)) : "memory");
    {
        const u32x4 sv = {SENT, SENT, SENT, SENT}, zv = {0u, 0u, 0u, 0u};
        u32x4* po = (u32x4*)(smem + K2_OFFS);
        constexpr int NO = 32 * LSTR / 4, NZ = (K2_PF - K2_EV) / 16;
        po[tid] = sv;
        if (tid < NO - 512) po[tid + 512] = sv;
        u32x4* pz = (u32x4*)(smem + K2_EV);
#pragma unroll
        for (int i = 0; i < NZ / 512; ++i) pz[tid + 512 * i] = zv;
        if (tid < NZ % 512) pz[tid + NZ / 512 * 512] = zv;
    }
    asm volatile("s_waitcnt vmcnt(0)" : "+v"(te), "+v"(cw0), "+v"(cw1), "+v"(cw2), "+v"(cw3) :: "memory");
    if (bin >= nbins) return;
    STAMP(1, 9);
    const int nc = (int)(te >> 16) - (int)j;
    u32x3 rec[SLOTS];
    unsigned long long vm[SLOTS];
#pragma unroll
    for (int i = 0; i < SLOTS; ++i) vm[i] = __builtin_amdgcn_ballot_w64(nc > TPC * i);
    {
        const rsrc_t rr = mk_rsrc(recs, (unsigned)E * 12u);
        const unsigned ro = (__umul24(c, ec) + (te & 0xffffu) + j) * 12u;
        asm volatile("buffer_load_dwordx3 %0, %5, %8, 0 offen\n\t"
                     "buffer_load_dwordx3 %1, %5, %8, 0 offen offset:144\n\t"
                     "buffer_load_dwordx3 %2, %5, %8, 0 offen offset:288\n\t"
                     "buffer_load_dwordx3 %3, %6, %8, 0 offen offset:432\n\t"
                     "buffer_load_dwordx3 %4, %7, %8, 0 offen offset:576"
                     : "=&v"(rec[0]), "=&v"(rec[1]), "=&v"(rec[2]), "=&v"(rec[3]), "=&v"(rec[4])
                     : "v"(ro), "v"(nc > TPC * 3 ? ro : OOB), "v"(nc > TPC * 4 ? ro : OOB), "s"(rr) : "memory");
    }
    const u32x2 cf = {pack_bf16(bf_lo(cw0) * NLOG2E, bf_lo(cw1) * NLOG2E), pack_bf16(bf_lo(cw2) * NLOG2E, bf_lo(cw3) * NLOG2E)};
    u32x4 asel;
#pragma unroll
    for (int k = 0; k < 4; ++k) asel[k] = (n == 4 * g + k) ? 0x3f80u : 0u;
    u32x2 lo16 = {(unsigned)n * 16u, (unsigned)n * 16u};
    unsigned vop = lds0 + K2_OFFS + (w * 4 + g) * (LSTR * 4);
    unsigned vep = lds0 + K2_EV + (((n & 3) == g) ? w * 4 + (n >> 2) : 32) * (LSTR * 8);
    u32x2 cfv = cf;
    asm volatile("" : "+v"(asel), "+v"(cfv), "+v"(lo16), "+v"(vop), "+v"(vep));
    __syncthreads();
    STAMP(1, 1);
    if (nc > TPC * SLOTS || force_slow) flag[0] = 1u;
    asm volatile("s_waitcnt vmcnt(0)" : "+v"(rec[0]), "+v"(rec[1]), "+v"(rec[2]), "+v"(rec[3]), "+v"(rec[4]) :: "memory");
    STAMP(1, 10);
    unsigned ca[SLOTS], rk[SLOTS];
#pragma unroll
    for (int i = 0; i < SLOTS; ++i) ca[i] = lds0 + K2_CNT + (w & 4) + (rec[i].z & 0xffffu);
#define K2_AT(R, A, M) "s_mov_b64 exec, %[" #M "]\n\tds_add_rtn_u32 %[" #R "], %[" #A "], %[one]\n\t"
    asm volatile(K2_AT(r0, a0, m0) K2_AT(r1, a1, m1) K2_AT(r2, a2, m2) K2_AT(r3, a3, m3) K2_AT(r4, a4, m4)
                 "s_mov_b64 exec, -1"
                 : [r0] "=&v"(rk[0]), [r1] "=&v"(rk[1]), [r2] "=&v"(rk[2]), [r3] "=&v"(rk[3]), [r4] "=&v"(rk[4])
                 : [a0] "v"(ca[0]), [a1] "v"(ca[1]), [a2] "v"(ca[2]), [a3] "v"(ca[3]), [a4] "v"(ca[4]),
                   [m0] "s"(vm[0]), [m1] "s"(vm[1]), [m2] "s"(vm[2]), [m3] "s"(vm[3]), [m4] "s"(vm[4]),
                   [one] "v"(1u << (8 * (w & 3)))
                 : "memory");
    asm volatile("s_waitcnt lgkmcnt(0)" : "+v"(rk[0]), "+v"(rk[1]), "+v"(rk[2]), "+v"(rk[3]), "+v"(rk[4]) :: "memory");
    __syncthreads();
    STAMP(1, 2);
    if (w >= 4) {
#pragma unroll
        for (int i = 0; i < 2 * EXP_PF; ++i)
            dma16(t1r, (b >> 3) * 4096u + (tid - 256) * 16u, hb * 4096u * i, lds0 + K2_PF + w * 1024);
    }

    unsigned pre[NBK], deg = 0, key = 0, rank = 0;
    if (tid < NB) {
        u32x2 f2[NBK];
#pragma unroll
        for (int k = 0; k < NBK; ++k) f2[k] = *(const u32x2*)(smem + K2_CNT + (k * NB + tid) * 16);
#pragma unroll
        for (int k = 0; k < NBK; ++k) {
            pre[k] = deg;
            deg = __builtin_amdgcn_sad_u8(f2[k][1], 0u, __builtin_amdgcn_sad_u8(f2[k][0], 0u, deg));
        }
        key = 63u - min(deg, 63u);
        rank = atomicAdd(&hist[key], 1u);
    }
    {
        unsigned long long bad = 0;
#pragma unroll
        for (int i = 0; i < SLOTS; ++i) {
            rk[i] = (rk[i] >> (8 * (w & 3))) & 255u;
            bad |= __builtin_amdgcn_ballot_w64(rk[i] > 63u) & vm[i];
        }
        if (bad) flag[0] = 1u;
    }
    __syncthreads();
    STAMP(1, 6);
    unsigned wv = 0, pos = 0;
    if (w < 2) {
        const unsigned hv = hist[lane];
        const unsigned ex = wave_scan(hv) - hv;
        rank += __builtin_amdgcn_ds_bpermute(key * 4, ex);
        pos = rank >> 4;
        wv = ((rank >> 2) & 3u) ^ ((pos & 1u) ? 3u : 0u);
        if (pos >= 2) { wv += 4; pos -= 2; }
        if (tid < NB) {
            smem[K2_ORD + wv * 16 + (rank & 3u) * 4 + pos] = (char)tid;
            if ((rank & 3u) == 0) smem[K2_LEN + wv * 4 + pos] = (char)min(max(deg, MINL), 255u);
        }
    }
    __syncthreads();
    STAMP(1, 7);
    if (tid < NB) {
        const unsigned l4 = *(const unsigned*)(smem + K2_LEN + wv * 4);
        const unsigned first = __builtin_amdgcn_sad_u8(l4 & ((1u << (8 * pos)) - 1u), 0u, 0u);
        const unsigned base = (wv * 4 + (rank & 3u)) * LSTR + first;
        if (first + max(deg, MINL) > MAXT) flag[0] = 1u;
#pragma unroll
        for (int k = 0; k < NBK; ++k) *(unsigned*)(smem + K2_CNT + (k * NB + tid) * 16 + 8) = base + pre[k];
    }
    unsigned lens = __builtin_amdgcn_readfirstlane(*(const unsigned*)(smem + K2_LEN + w * 4));
    unsigned ordb = *(const unsigned*)(smem + K2_ORD + w * 16 + g * 4);
    const rsrc_t t2r = mk_rsrc(T2 + ((size_t)s * N + node0) * FS, (unsigned)(N - node0) * 256u);
    __syncthreads();
    STAMP(1, 8);

    if (flag[0]) {
        WAIT_V(0);
        k2_slow(smem, T1, T2, gw, ea, src, dst, out, N, E, s, node0);
        return;
    }

    {
        const unsigned lowm = (1u << (8 * (w & 3))) - 1u;
        const unsigned m0 = (w & 4) ? 0xffffffffu : lowm, m1 = (w & 4) ? lowm : 0u;
        u32x4 cs[SLOTS];
#pragma unroll
        for (int i = 0; i < SLOTS; ++i) cs[i] = *(const u32x4*)(smem + K2_CNT + (rec[i].z & 0xffffu));
        __builtin_amdgcn_sched_barrier(0);
#pragma unroll
        for (int i = 0; i < SLOTS; ++i) {
            const unsigned p = __builtin_amdgcn_sad_u8(cs[i][1] & m1, 0u, __builtin_amdgcn_sad_u8(cs[i][0] & m0, 0u, rk[i])) + cs[i][2];
            const unsigned ov = (rec[i].z >> 16) << 8;
            asm volatile("s_mov_b64 exec, %4\n\tds_write_b32 %0, %1 offset:%c5\n\tds_write_b64 %2, %3 offset:%c6\n\ts_mov_b64 exec, -1"
                         :: "v"(p * 4), "v"(ov), "v"(p * 8), "v"(u32x2{rec[i].x, rec[i].y}), "s"(vm[i]), "i"(K2_OFFS), "i"(K2_EV) : "memory");
        }
    }
    const rsrc_t outr = mk_rsrc(out + (size_t)node0 * D + s * FS, (unsigned)(N - node0) * 512u - s * 128);
    WAIT_L0();
    __syncthreads();
    STAMP(1, 3);
    unsigned tot = (lens & 255) + ((lens >> 8) & 255) + ((lens >> 16) & 255);

    BLK_DECL;
#define K2_STEP(K, D0, D1, D2, D3, DN, Z0, Z1, Z2, Z3, ZN, EK, CF, AK, PRE, MID, POST)   \
        "s_sub_u32 s66, s66, 1\n\t"                                         \
        "s_cbranch_scc1 .Lsw" #K "_%=\n"                                          \
        ".Lrt" #K "_%=:\n\t"                                                      \
        "s_waitcnt lgkmcnt(2)\n\t"                                                \
        PRE                                                                       \
        AB_M("v_mfma_f32_16x16x32_bf16 " ZN ", " EK ", " CF ", v[18:21]\n\t")     \
        MID                                                                       \
        "v_exp_f32 v" #Z0 ", v" #Z0 "\n\t"                                        \
        "v_exp_f32 v" #Z1 ", v" #Z1 "\n\t"                                        \
        "v_exp_f32 v" #Z2 ", v" #Z2 "\n\t"                                        \
        "v_exp_f32 v" #Z3 ", v" #Z3 "\n\t"                                        \
        "v_pk_add_f32 v[" #Z0 ":" #Z1 "], v[" #Z0 ":" #Z1 "], 1.0 op_sel_hi:[1,0]\n\t" \
        "v_pk_add_f32 v[" #Z2 ":" #Z3 "], v[" #Z2 ":" #Z3 "], 1.0 op_sel_hi:[1,0]\n\t" \
        AB_G("s_waitcnt vmcnt(6)\n\t")                                            \
        AB_M("v_mfma_f32_16x16x32_bf16 " ZN ", v[8:11], " DN ", " ZN "\n\t")      \
        AB_T("v_rcp_f32 v" #Z0 ", v" #Z0 "\n\t")                                  \
        AB_T("v_rcp_f32 v" #Z1 ", v" #Z1 "\n\t")                                  \
        AB_T("v_rcp_f32 v" #Z2 ", v" #Z2 "\n\t")                                  \
        AB_T("v_rcp_f32 v" #Z3 ", v" #Z3 "\n\t")                                  \
        "v_pk_fma_f32 v[22:23], v[" #Z0 ":" #Z1 "], v[" #D0 ":" #D1 "], v[22:23]\n\t" \
        "v_pk_fma_f32 v[24:25], v[" #Z2 ":" #Z3 "], v[" #D2 ":" #D3 "], v[24:25]\n\t" \
        AB_G("buffer_load_dwordx4 v[" #D0 ":" #D3 "], " AK ", %[t1r], 0 offen\n\t") \
        POST
#define K2_SW(K) ".Lsw" #K "_%=:\n\ts_swappc_b64 s[62:63], s[60:61]\n\ts_branch .Lrt" #K "_%=\n"
#define K2_ADDA "v_pk_add_f32 v[46:47], v[46:47], v[50:51]\n\t"
#define K2_ADDB "v_pk_add_f32 v[48:49], v[48:49], v[50:51]\n\t"
#define K2_EA(O) AB_E("ds_read_b128 v[38:41], v53 offset:" #O "\n\t")
#define K2_EB(O) AB_E("ds_read_b128 v[42:45], v53 offset:" #O "\n\t")
#define K2_AA(O) "ds_read_b64 v[46:47], v52 offset:" #O "\n\t"
#define K2_AB(O) "ds_read_b64 v[48:49], v52 offset:" #O "\n\t"
#define K2_NEXT_T2                                                                \
        "v_and_b32 v57, 0xff, v54\n\t"                                            \
        "v_lshrrev_b32 v54, 8, v54\n\t"                                           \
        "v_lshl_add_u32 v58, v57, 8, v50\n\t"                                     \
        "v_lshl_add_u32 v59, v57, 9, v7\n\t"                                      \
        "buffer_load_dwordx4 v[26:29], v58, %[t2r], 0 offen\n\t"
#define K2_CIN  "v_lshlrev_b32 v18, 16, v26\n\tv_lshlrev_b32 v19, 16, v27\n\tv_lshlrev_b32 v20, 16, v28\n\tv_lshlrev_b32 v21, 16, v29\n\t"
#define K2_ACC  "v_and_b32 v22, 0xffff0000, v26\n\tv_and_b32 v23, 0xffff0000, v27\n\tv_and_b32 v24, 0xffff0000, v28\n\tv_and_b32 v25, 0xffff0000, v29\n\tv_mov_b32 v56, v59\n\t"
#define K2_LD(D0, D3, A) "buffer_load_dwordx4 v[" #D0 ":" #D3 "], v" #A ", %[t1r], 0 offen\n\t"
    asm volatile(
        "s_waitcnt lgkmcnt(0)\n\t"
        "ds_read_b128 v[30:33], v52\n\t"
        "ds_read_b128 v[34:37], v52 offset:16\n\t"
        K2_NEXT_T2
        K2_EA(0) K2_AA(32) K2_EB(16) K2_AB(40)
        "s_getpc_b64 s[60:61]\n"
        ".Lpc_%=:\n\t"
        "s_add_u32 s60, s60, .Lhnd_%=-.Lpc_%=\n\t"
        "s_addc_u32 s61, s61, 0\n\t"
        "s_and_b32 s66, s64, 0xff\n\t"
        "s_sub_u32 s66, s66, 1\n\t"
        "s_mov_b32 s67, 0\n\t"
        "s_mov_b32 s68, 0xbfb8aa3b\n\t"
        "v_mov_b32 v12, 0\n\t"
        "v_mov_b32 v13, 0\n\t"
        "v_mov_b32 v16, 0\n\t"
        "v_mov_b32 v17, 0\n\t"
        "s_waitcnt lgkmcnt(4)\n\t"
        "v_pk_add_f32 v[30:31], v[30:31], v[50:51]\n\t"
        "v_pk_add_f32 v[32:33], v[32:33], v[50:51]\n\t"
        "v_pk_add_f32 v[34:35], v[34:35], v[50:51]\n\t"
        "v_pk_add_f32 v[36:37], v[36:37], v[50:51]\n\t"
        K2_LD(64, 67, 30) K2_LD(68, 71, 31) K2_LD(72, 75, 32) K2_LD(76, 79, 33)
        K2_LD(80, 83, 34) K2_LD(84, 87, 35) K2_LD(88, 91, 36) K2_LD(92, 95, 37)
        "s_setprio 2\n\t"
        "s_waitcnt vmcnt(8) lgkmcnt(3)\n\t"
        K2_CIN K2_ACC
        K2_NEXT_T2
        "v_mfma_f32_16x16x32_bf16 v[30:33], v[38:41], v[14:17], v[18:21]\n\t"
        "s_waitcnt vmcnt(8)\n\t"
        "v_mfma_f32_16x16x32_bf16 v[30:33], v[8:11], v[64:67], v[30:33]\n\t"
        BLK_INIT
        "s_nop 3\n"
        ".Lloop_%=:\n\t"
        K2_STEP(0, 64, 65, 66, 67, "v[68:71]", 30, 31, 32, 33, "v[34:37]", "v[38:41]", "v[12:15]", "v46", K2_ADDA, K2_EA(32), "")
        K2_STEP(1, 68, 69, 70, 71, "v[72:75]", 34, 35, 36, 37, "v[30:33]", "v[42:45]", "v[14:17]", "v47", "", "", K2_AA(48))
        K2_STEP(2, 72, 73, 74, 75, "v[76:79]", 30, 31, 32, 33, "v[34:37]", "v[42:45]", "v[12:15]", "v48", K2_ADDB, K2_EB(48), "")
        K2_STEP(3, 76, 77, 78, 79, "v[80:83]", 34, 35, 36, 37, "v[30:33]", "v[38:41]", "v[14:17]", "v49", "", "", K2_AB(56))
        K2_STEP(4, 80, 81, 82, 83, "v[84:87]", 30, 31, 32, 33, "v[34:37]", "v[38:41]", "v[12:15]", "v46", K2_ADDA, K2_EA(64), "")
        K2_STEP(5, 84, 85, 86, 87, "v[88:91]", 34, 35, 36, 37, "v[30:33]", "v[42:45]", "v[14:17]", "v47", "", "", K2_AA(64))
        K2_STEP(6, 88, 89, 90, 91, "v[92:95]", 30, 31, 32, 33, "v[34:37]", "v[42:45]", "v[12:15]", "v48", K2_ADDB, K2_EB(80), "")
        K2_STEP(7, 92, 93, 94, 95, "v[64:67]", 34, 35, 36, 37, "v[30:33]", "v[38:41]", "v[14:17]", "v49", "", "", K2_AB(72))
        "v_add_u32 v53, 64, v53\n\t"
        "v_add_u32 v52, 32, v52\n\t"
        BLK_STAMP
        "s_sub_u32 s65, s65, 8\n\t"
        "s_cmp_gt_i32 s65, %c[p1]\n\t"
        "s_cbranch_scc1 .Lloop_%=\n\t"
        "s_setprio 1\n\t"
        "s_cmp_gt_i32 s65, %c[p2]\n\t"
        "s_cbranch_scc1 .Lloop_%=\n\t"
        "s_setprio 0\n\t"
        "s_branch .Lloop_%=\n"
        K2_SW(0) K2_SW(1) K2_SW(2) K2_SW(3) K2_SW(4) K2_SW(5) K2_SW(6) K2_SW(7)
        ".Lhnd_%=:\n\t"
        "s_cmp_lg_u32 s67, 0\n\t"
        "s_cbranch_scc1 .Lflush_%=\n\t"
        K2_CIN
        "s_mov_b32 s66, 0\n\t"
        "s_mov_b32 s67, 1\n\t"
        "s_setpc_b64 s[62:63]\n"
        ".Lflush_%=:\n\t"
        "v_pk_mul_f32 v[60:61], v[22:23], s[68:69] op_sel_hi:[1,0]\n\t"
        "v_pk_mul_f32 v[62:63], v[24:25], s[68:69] op_sel_hi:[1,0]\n\t"
        "v_exp_f32 v60, v60\n\t"
        "v_exp_f32 v61, v61\n\t"
        "v_exp_f32 v62, v62\n\t"
        "v_exp_f32 v63, v63\n\t"
        "v_pk_add_f32 v[60:61], v[60:61], 1.0 op_sel_hi:[1,0]\n\t"
        "v_pk_add_f32 v[62:63], v[62:63], 1.0 op_sel_hi:[1,0]\n\t"
        "v_rcp_f32 v60, v60\n\t"
        "v_rcp_f32 v61, v61\n\t"
        "v_rcp_f32 v62, v62\n\t"
        "v_rcp_f32 v63, v63\n\t"
        "v_pk_mul_f32 v[60:61], v[60:61], v[22:23]\n\t"
        "v_pk_mul_f32 v[62:63], v[62:63], v[24:25]\n\t"
        "v_cvt_pk_bf16_f32 v62, v60, v62\n\t"
        "v_cvt_pk_bf16_f32 v63, v61, v63\n\t"
        "s_mov_b32 vcc_lo, 0x55555555\n\t"
        "s_mov_b32 vcc_hi, 0x55555555\n\t"
        "v_cndmask_b32_dpp v60, v63, v62, vcc quad_perm:[1,0,3,2] row_mask:0xf bank_mask:0xf\n\t"
        "s_not_b64 vcc, vcc\n\t"
        "v_cndmask_b32_dpp v61, v62, v63, vcc quad_perm:[1,0,3,2] row_mask:0xf bank_mask:0xf\n\t"
        "s_lshr_b32 s64, s64, 8\n\t"
        "buffer_store_dwordx2 v[60:61], v56, %[outr], 0 offen\n\t"
        "s_and_b32 s66, s64, 0xff\n\t"
        "s_cbranch_scc0 .Ldone_%=\n\t"
        K2_ACC
        K2_NEXT_T2
        "s_sub_u32 s66, s66, 2\n\t"
        "s_mov_b32 s67, 0\n\t"
        "s_setpc_b64 s[62:63]\n"
        ".Ldone_%=:\n\t"
        "s_waitcnt vmcnt(0) lgkmcnt(0)\n\t"
        BLK_LAST
        : BLK_OUT "+{s64}"(lens), "+{s65}"(tot), "+{v52}"(vop), "+{v53}"(vep), "+{v54}"(ordb)
        : [p1] "i"(EXP_P1), [p2] "i"(EXP_P2), [t1r] "s"(t1r), [t2r] "s"(t2r), [outr] "s"(outr), "{v[8:11]}"(asel), "{v[14:15]}"(cfv), "{v[50:51]}"(lo16),
          "{v7}"((unsigned)((n >> 3) * 64 + (n & 6) * 4 + (n & 1) * 32))
        : "v12", "v13", "v16", "v17", "v18", "v19", "v20", "v21", "v22", "v23", "v24", "v25", "v26", "v27", "v28", "v29",
          "v30", "v31", "v32", "v33", "v34", "v35", "v36", "v37", "v38", "v39", "v40", "v41", "v42", "v43", "v44", "v45",
          "v46", "v47", "v48", "v49", "v56", "v57", "v58", "v59", "v60", "v61", "v62", "v63",
          "v64", "v65", "v66", "v67", "v68", "v69", "v70", "v71", "v72", "v73", "v74", "v75", "v76", "v77", "v78", "v79",
          "v80", "v81", "v82", "v83", "v84", "v85", "v86", "v87", "v88", "v89", "v90", "v91", "v92", "v93", "v94", "v95",
          "s60", "s61", "s62", "s63", "s66", "s67", "s68", "s69", "s70", "s71", "vcc", "scc", "memory");
    BLK_FLUSH;
    STAMP(1, 5);
    STAMP_FLUSH(1, 5);
    WSTAMP(0, stv[3]);
    WSTAMP(1, stv[5]);
    WSTAMP(2, tsteps);
    WSTAMP(3, (unsigned long long)__builtin_amdgcn_s_getreg(63492) | ((unsigned long long)__builtin_amdgcn_s_getreg(63508) << 32));
}

}

extern "C" void kernel_launch(void* const* d_in, const int* in_sizes, int n_in,
                              void* d_out, int out_size, void* d_ws, size_t ws_size,
                              hipStream_t stream) {
    const uint16_t* x  = (const uint16_t*)d_in[0];
    const uint16_t* ea = (const uint16_t*)d_in[1];
    const int* src     = (const int*)d_in[2];
    const int* dst     = (const int*)d_in[3];
    const uint16_t* uw = (const uint16_t*)d_in[4];
    const uint16_t* vw = (const uint16_t*)d_in[5];
    const uint16_t* gw = (const uint16_t*)d_in[6];
    const int N = in_sizes[0] / D;
    const int E = in_sizes[2];
    const int nrb   = (N + BM - 1) / BM;
    const int nbins = (N + NB - 1) / NB;
    int nchunks = 40;
    const int slots = nrb + nchunks / 8;
    const int ec = ((E + nchunks - 1) / nchunks + 3) & ~3;
    const int force_slow = (nbins > BINMAX || ec > RMAX * 2048) ? 1 : 0;
    if (force_slow) nchunks = 0;

    char* ws = (char*)d_ws;
    const size_t tb = ((size_t)NS * N * FS * 4 + 255) & ~(size_t)255;
    unsigned* T1 = (unsigned*)ws;
    unsigned* T2 = (unsigned*)(ws + tb);
    unsigned* recs = (unsigned*)(ws + 2 * tb);
    unsigned* tab = (unsigned*)(ws + 2 * tb + (((size_t)E * 12 + 255) & ~(size_t)255));

    (void)hipFuncSetAttribute((const void*)egc_k1, hipFuncAttributeMaxDynamicSharedMemorySize, K1_LDS);
    egc_k1<<<slots * 8, 512, K1_LDS, stream>>>(x, ea, src, dst, uw, vw, gw, T1, T2, recs, tab,
                                                 N, E, nrb, nbins, ec, nchunks, (N + nrb - 1) / nrb, NBK * 65536 / N);
    egc_k2<<<((nbins + 1) / 2) * 8, 512, 0, stream>>>(T1, T2, recs, tab, gw, ea, src, dst, (uint16_t*)d_out,
                                                      N, E, nchunks, nbins, ec, force_slow);
}
```

```cpp
#include <hip/hip_runtime.h>
#include <hip/hip_bf16.h>
#include <stdint.h>

typedef float    f32x4 __attribute__((ext_vector_type(4)));
typedef float    f32x2 __attribute__((ext_vector_type(2)));
typedef short    s16x4 __attribute__((ext_vector_type(4)));
typedef __bf16   bf16x8 __attribute__((ext_vector_type(8)));
typedef __bf16   bf16x2 __attribute__((ext_vector_type(2)));
typedef unsigned u32x4 __attribute__((ext_vector_type(4)));
typedef unsigned u32x3 __attribute__((ext_vector_type(3)));
typedef unsigned u32x2 __attribute__((ext_vector_type(2)));
typedef __amdgpu_buffer_rsrc_t rsrc_t;
#define LDSP __attribute__((address_space(3)))

#define BLK_DECL
#define BLK_INIT
#define BLK_STAMP
#define BLK_LAST
#define BLK_OUT
#define BLK_FLUSH
#define STAMP_DECL
#define STAMP_ARG
#define STAMP_PASS
#define STAMP_FLUSH(K, L) do {} while (0)
#define STAMP(K, I) do {} while (0)
#define WSTAMP(I, V) do {} while (0)

#define AB_M(x) x
#define AB_G(x) x
#define AB_E(x) x
#define AB_T(x) x
#define NBK 4
#define EXP_PF 2
#define EXP_P1 20
#define EXP_P2 10
#define EXP_ROT 0
namespace {

constexpr int D     = 256;
constexpr int FS    = 64;
constexpr int NS    = 4;
constexpr int BM    = 384;
constexpr int NB    = 80;
constexpr int CMAX  = 42;
constexpr int TSTR  = 64;
constexpr int RMAX  = 2;
constexpr int BINMAX = 256;
constexpr int SLOTS = 5;
constexpr int TPC   = 12;
constexpr int LSTR  = 72;
constexpr int MAXT  = 59;
constexpr unsigned MINL = 8;
constexpr float NLOG2E = -1.4426950408889634f;
constexpr unsigned SENT = 0x00fff000u;
constexpr unsigned OOB  = 0x7ffffff0u;
constexpr int AUX_WT = 16;

__device__ __forceinline__ rsrc_t mk_rsrc(const void* p, unsigned bytes) {
    return __builtin_amdgcn_make_buffer_rsrc((void*)p, (short)0, (int)bytes, 0x00020000);
}
__device__ __forceinline__ void dma16(rsrc_t r, unsigned voff, unsigned soff, unsigned lds) {
    asm volatile("s_mov_b32 m0, %0\n\ts_nop 0\n\tbuffer_load_dwordx4 %1, %2, %3 offen lds"
                 :: "s"(lds), "v"(voff), "s"(r), "s"(soff) : "m0", "memory");
}
#define WAIT_V(n) asm volatile("s_waitcnt vmcnt(%0)" :: "i"(n) : "memory")
#define WAIT_L0() asm volatile("s_waitcnt lgkmcnt(0)" ::: "memory")
#define BAR() __builtin_amdgcn_s_barrier()

__device__ __forceinline__ unsigned wave_scan(unsigned v) {
    v += __builtin_amdgcn_update_dpp(0u, v, 0x111, 0xf, 0xf, false);
    v += __builtin_amdgcn_update_dpp(0u, v, 0x112, 0xf, 0xf, false);
    v += __builtin_amdgcn_update_dpp(0u, v, 0x114, 0xf, 0xf, false);
    v += __builtin_amdgcn_update_dpp(0u, v, 0x118, 0xf, 0xf, false);
    v += __builtin_amdgcn_update_dpp(0u, v, 0x142, 0xa, 0xf, false);
    v += __builtin_amdgcn_update_dpp(0u, v, 0x143, 0xc, 0xf, false);
    return v;
}
__device__ __forceinline__ unsigned pack_bf16(float lo, float hi) {
    f32x2 p = {lo, hi};
    return __builtin_bit_cast(unsigned, __builtin_convertvector(p, bf16x2));
}
__device__ __forceinline__ float bf_lo(unsigned v) { return __builtin_bit_cast(float, v << 16); }
__device__ __forceinline__ float bf_hi(unsigned v) { return __builtin_bit_cast(float, v & 0xffff0000u); }

__device__ __forceinline__ unsigned pack_t1(float xb, float hv) {
    const float p = xb * NLOG2E;
    unsigned P;
    asm("v_cvt_pk_bf16_f32 %0, %1, -%1" : "=v"(P) : "v"(p));
    const unsigned t = __builtin_bit_cast(unsigned, hv) - (unsigned)((int)P >> 16);
    return (t & 0xffff0000u) | (P & 0xffffu);
}

constexpr int K1_W   = 98304;
constexpr int K1_LDS = 163840;
__host__ __device__ constexpr int K1_R(int r) { return r == 5 ? 0 : r * 32768; }

template <int T>
__device__ __forceinline__ unsigned k1_pack1(float a, float b) {
    asm("" : "+v"(a));
    return T ? pack_bf16(a * NLOG2E, b) : pack_t1(a, b);
}

template <int T>
__device__ __forceinline__ void k1_gemm(char* smem, const unsigned lds0, const int lane, const int w, const int s, const int rot,
                                        const rsrc_t xr, const rsrc_t wr, const rsrc_t tr STAMP_ARG) {
    const int g = lane >> 4, n = lane & 15;
    const int fh = w & 1, rg = w >> 1, u = w & 3;
    const unsigned rl = 2 * w + (lane >> 5);
    const unsigned xvo = rl * 512 + (((lane & 31) ^ rl) << 4);
    const unsigned f = ((lane >> 4) & 1) | ((u & 1) << 1);
    const unsigned wvo = (lane >> 3) * 512 + s * 128 + ((((lane >> 1) & 3) ^ f) << 5) + (lane & 1) * 16 + u * 4096;
    const unsigned ldw = lds0 + w * 1024;
    unsigned gr[6];
    gr[0] = 0;
#pragma unroll
    for (int r = 1; r < 6; ++r) gr[r] = (rot + r > 5) ? rot + r - 5 : rot + r;
#define K1_LDW(ks)   dma16(wr, wvo, (ks) * 16384, ldw + K1_W + (ks) * 8192)
#define K1_LDX(r, i) dma16(xr, xvo, gr[r] * 32768 + (i) * 8192, ldw + K1_R(r) + (i) * 8192)
#pragma unroll
    for (int ks = 0; ks < 8; ++ks) K1_LDW(ks);
#pragma unroll
    for (int i = 0; i < 4; ++i) K1_LDX(0, i);
    STAMP(0, 1);

    const float z1 = T ? 0.f : 1e-36f;
    const f32x4 zero4 = {0.f, 0.f, 0.f, 0.f}, tiny4 = {z1, z1, z1, z1};
    bf16x8 wf[8][4];
    bf16x8 xq[8];
    f32x4 acc[2][4];
    const unsigned fr = ((n >> 3) & 1) | ((g & 1) << 1);
    const char* wp0 = smem + K1_W + (8 * g + (n >> 2)) * 128 + (((2 * fh) ^ fr) << 5) + 8 * (n & 3);
    const char* wp1 = smem + K1_W + (8 * g + (n >> 2)) * 128 + (((2 * fh + 1) ^ fr) << 5) + 8 * (n & 3);
    unsigned xa[3][4];
#pragma unroll
    for (int k = 0; k < 4; ++k) {
        xa[0][k] = lds0 + n * 512 + rg * 8192 + (((4 * k + g) ^ n) << 4);
        xa[1][k] = xa[0][k] + 65536; xa[2][k] = xa[0][k] + 131072;
    }
    const unsigned ovo = g * 1024 + fh * 128 + n * 8 + rg * 4096;
#define K1_RDW(ks) do { _Pragma("unroll") for (int mf = 0; mf < 4; ++mf) {                                   \
        const char* wp = ((mf & 1) ? wp1 : wp0) + (ks) * 8192 + (mf >> 1) * 4096;                           \
        u32x2 l2 = __builtin_bit_cast(u32x2, __builtin_amdgcn_ds_read_tr16_b64_v4i16((LDSP s16x4*)wp));      \
        u32x2 h2 = __builtin_bit_cast(u32x2, __builtin_amdgcn_ds_read_tr16_b64_v4i16((LDSP s16x4*)(wp + 512))); \
        wf[ks][mf] = __builtin_bit_cast(bf16x8, u32x4{l2.x, l2.y, h2.x, h2.y}); } } while (0)
#define K1_RDX(r, ks) xq[ks] = *(const LDSP bf16x8*)(size_t)(xa[K1_R(r) >> 16][(ks) & 3] + (K1_R(r) & 65535) + ((ks) >> 2) * 256)
#define K1_MM(p, ks) do { _Pragma("unroll") for (int m = 0; m < 4; ++m)                                      \
        acc[p][m] = __builtin_amdgcn_mfma_f32_16x16x32_bf16(xq[ks], wf[ks][m], (ks) ? acc[p][m] : (m < 2 ? zero4 : tiny4), 0, 0, 0); } while (0)
#define SB() __builtin_amdgcn_sched_barrier(0)

    WAIT_V(4); BAR();
    STAMP(0, 2);
#pragma unroll
    for (int ks = 0; ks < 4; ++ks) { K1_RDW(ks); K1_LDX(1 + (ks >> 1), (ks & 1) * 2); K1_LDX(1 + (ks >> 1), (ks & 1) * 2 + 1); }
    SB();
    STAMP(0, 14);
    WAIT_V(8); BAR();
    STAMP(0, 15);
#pragma unroll
    for (int ks = 0; ks < 8; ++ks) K1_RDX(0, ks);
    SB();
#pragma unroll
    for (int ks = 0; ks < 4; ++ks) { K1_RDW(4 + ks); K1_MM(0, ks); SB(); }
    WAIT_L0(); WAIT_V(4); BAR();
    STAMP(0, 3);
#pragma unroll
    for (int ks = 0; ks < 4; ++ks) K1_RDX(1, ks);
    SB();
#pragma unroll
    for (int ks = 4; ks < 8; ++ks) { K1_MM(0, ks); K1_RDX(1, ks); K1_LDX(3, ks - 4); SB(); }

#pragma unroll
    for (int r = 1; r < 6; ++r) {
        const int p = r & 1, q = p ^ 1;
        if (r == 1) { WAIT_V(4); BAR(); STAMP(0, 4); }
        if (r == 2) { STAMP(0, 7); WAIT_V(12); BAR(); STAMP(0, 8); }
        if (r == 3) { WAIT_V(4); BAR(); STAMP(0, 9); }
        if (r == 4) STAMP(0, 11);
        if (r == 5) STAMP(0, 12);
        const unsigned vo = ovo + gr[r - 1] * 16384;
        u32x2 o;
#pragma unroll
        for (int ks = 0; ks < 8; ++ks) {
            K1_MM(p, ks);
            if (r < 5) K1_RDX(r + 1, ks);
            o[ks & 1] = k1_pack1<T>(acc[q][ks & 1][ks >> 1], acc[q][2 + (ks & 1)][ks >> 1]);
            if (ks & 1) __builtin_amdgcn_raw_buffer_store_b64(o, tr, vo + (ks >> 1) * 256, 0, AUX_WT);
            if (r == 1) K1_LDX(4 + (ks >> 2), ks & 3);
#pragma unroll
            for (int i = 0; i < 4; ++i) {
                __builtin_amdgcn_sched_group_barrier(0x008, 1, 0);
                __builtin_amdgcn_sched_group_barrier(0x002, 1, 0);
            }
            SB();
        }
    }
    STAMP(0, 13);
#pragma unroll
    for (int r = 0; r < 4; ++r) {
        const u32x2 o = {k1_pack1<T>(acc[1][0][r], acc[1][2][r]), k1_pack1<T>(acc[1][1][r], acc[1][3][r])};
        __builtin_amdgcn_raw_buffer_store_b64(o, tr, ovo + gr[5] * 16384 + r * 256, 0, AUX_WT);
    }
#undef K1_LDW
#undef K1_LDX
}

__global__ __launch_bounds__(512, 2)
void egc_k1(const uint16_t* __restrict__ x, const uint16_t* __restrict__ ea,
            const int* __restrict__ src, const int* __restrict__ dst,
            const uint16_t* __restrict__ uw, const uint16_t* __restrict__ vw,
            const uint16_t* __restrict__ gw,
            unsigned* __restrict__ T1, unsigned* __restrict__ T2,
            unsigned* __restrict__ recs, unsigned* __restrict__ tab,
            int N, int E, int nrb, int nbins, int ec, int nchunks, int rows, int bmul)
{
    extern __shared__ __attribute__((aligned(16))) char smem[];
    const int b   = blockIdx.x;
    const int slot = b >> 3;
    const int item = (b & 7) * nrb + slot;
    const int rb  = item >> 3;
    const int ct  = item & 7;
    const int tid = threadIdx.x;
    const int lane = tid & 63;
    const int w = __builtin_amdgcn_readfirstlane(tid >> 6);
    asm volatile("" :: "s"(x), "s"(ea), "s"(src), "s"(dst), "s"(uw), "s"(vw), "s"(gw), "s"(T1), "s"(T2), "s"(recs), "s"(tab),
                       "s"(N), "s"(E), "s"(nrb), "s"(nbins), "s"(ec), "s"(nchunks), "s"(rows), "s"(bmul));
    STAMP_DECL;
    STAMP(0, 0);

    if (slot >= nrb) {
        const int c = (slot - nrb) * 8 + (b & 7);
        if (c >= nchunks) return;
        unsigned* cw  = (unsigned*)smem;
        unsigned* wt  = cw + 8 * BINMAX;
        unsigned* rec = wt + 16;
        const int e0 = c * ec;
        const int ne = max(min(E - e0, ec), 0);
        const rsrc_t dr = mk_rsrc(dst + e0, ne * 4), sr = mk_rsrc(src + e0, ne * 4), er = mk_rsrc(ea + (size_t)e0 * 4, ne * 8);
        u32x4 dv[RMAX], sv[RMAX], ev[RMAX][2];
#pragma unroll
        for (int r = 0; r < RMAX; ++r) dv[r] = __builtin_amdgcn_raw_buffer_load_b128(dr, tid * 16, r * 8192, 0);
        ((u32x4*)cw)[tid] = u32x4{0u, 0u, 0u, 0u};
#pragma unroll
        for (int r = 0; r < RMAX; ++r) {
            sv[r] = __builtin_amdgcn_raw_buffer_load_b128(sr, tid * 16, r * 8192, 0);
            ev[r][0] = __builtin_amdgcn_raw_buffer_load_b128(er, tid * 32, r * 16384, 0);
            ev[r][1] = __builtin_amdgcn_raw_buffer_load_b128(er, tid * 32 + 16, r * 16384, 0);
        }
        __builtin_amdgcn_sched_barrier(0);
        __syncthreads();
        unsigned* cmine = cw + w * BINMAX;
        unsigned rk[RMAX][4], bn[RMAX][4];
#pragma unroll
        for (int r = 0; r < RMAX; ++r)
#pragma unroll
            for (int k = 0; k < 4; ++k) {
                bn[r][k] = __umul24(dv[r][k], 52429u) >> 22;
                rk[r][k] = 0;
                if (r * 2048 + tid * 4 + k < ne) rk[r][k] = atomicAdd(&cmine[bn[r][k]], 1u);
            }
        __syncthreads();
        STAMP(2, 1);
        {
            unsigned pre[8], t = 0, inc = 0;
            if (tid < BINMAX) {
#pragma unroll
                for (int k = 0; k < 8; ++k) { pre[k] = t; t += cw[k * BINMAX + tid]; }
                inc = wave_scan(t);
                if (lane == 63) wt[w] = inc;
            }
            __syncthreads();
            if (tid < BINMAX) {
                const u32x4 w4 = *(const u32x4*)wt;
                const unsigned st = inc - t + (w > 0 ? w4[0] : 0u) + (w > 1 ? w4[1] : 0u) + (w > 2 ? w4[2] : 0u);
#pragma unroll
                for (int k = 0; k < 8; ++k) cw[k * BINMAX + tid] = st + pre[k];
                if (tid < nbins) tab[tid * TSTR + c] = st | (t << 16);
            }
        }
        __syncthreads();
        STAMP(2, 2);
#pragma unroll
        for (int r = 0; r < RMAX; ++r)
#pragma unroll
            for (int k = 0; k < 4; ++k) rk[r][k] += cmine[bn[r][k]];
#pragma unroll
        for (int r = 0; r < RMAX; ++r)
#pragma unroll
            for (int k = 0; k < 4; ++k)
                if (r * 2048 + tid * 4 + k < ne) {
                    unsigned* p = rec + rk[r][k] * 3;
                    p[0] = ev[r][k >> 1][(k & 1) * 2];
                    p[1] = ev[r][k >> 1][(k & 1) * 2 + 1];
                    p[2] = (sv[r][k] << 16) + (dv[r][k] + ((__umul24(sv[r][k], bmul) >> 16) - bn[r][k]) * NB) * 16u;
                }
        __syncthreads();
        STAMP(2, 3);
        const rsrc_t orr = mk_rsrc(recs + (size_t)e0 * 3, ne * 12);
#pragma unroll
        for (int i = 0; i < RMAX * 3; ++i) {
            if (i * 8192 < ne * 12) {
                u32x4 v = ((const u32x4*)rec)[tid + 512 * i];
                __builtin_amdgcn_raw_buffer_store_b128(v, orr, tid * 16 + i * 8192, 0, AUX_WT);
            }
        }
        STAMP(2, 5);
        STAMP(2, 6);
        STAMP_FLUSH(2, 6);
        return;
    }

    const int s = ct >> 1;
    const int row0 = rb * rows;
    const unsigned nr = (unsigned)max(min(rows, N - row0), 0);
    const unsigned lds0 = (unsigned)(size_t)(LDSP char*)smem;
    const rsrc_t xr = mk_rsrc(x + (size_t)row0 * D, nr * 512u);
    const size_t to = ((size_t)s * N + row0) * FS;
    if (ct & 1)
        k1_gemm<1>(smem, lds0, lane, w, s, ct >= 5 ? ct - 5 : ct, xr, mk_rsrc(w < 4 ? gw : uw, D * D * 2), mk_rsrc(T2 + to, nr * 256u) STAMP_PASS);
    else
        k1_gemm<0>(smem, lds0, lane, w, s, ct >= 5 ? ct - 5 : ct, xr, mk_rsrc(w < 4 ? gw + D * D : vw, D * D * 2), mk_rsrc(T1 + to, nr * 256u) STAMP_PASS);
    STAMP(0, 5);
    STAMP(0, 6);
    STAMP_FLUSH(0, 6);
}

constexpr int K2_OFFS = 0;
constexpr int K2_EV   = K2_OFFS + 32 * LSTR * 4;
constexpr int K2_CNT  = K2_EV + 33 * LSTR * 8;
constexpr int K2_HIST = K2_CNT + NBK * NB * 16;
constexpr int K2_ORD  = K2_HIST + 256;
constexpr int K2_LEN  = K2_ORD + 128;
constexpr int K2_FLAG = K2_LEN + 32;
constexpr int K2_PF   = K2_FLAG + 16;
constexpr int K2_LDS  = K2_PF + 8192;
static_assert((K2_PF - K2_EV) % 16 == 0 && K2_EV % 16 == 0, "LDS map");

__device__ __forceinline__ u32x2 silu_pack(f32x4 y) {
    f32x4 o;
#pragma unroll
    for (int r = 0; r < 4; ++r) {
        float t = __builtin_amdgcn_exp2f(y[r] * NLOG2E);
        o[r] = y[r] * __builtin_amdgcn_rcpf(1.0f + t);
    }
    return u32x2{pack_bf16(o[0], o[1]), pack_bf16(o[2], o[3])};
}

__device__ __forceinline__ void k2_slow(char* smem, const unsigned* __restrict__ T1, const unsigned* __restrict__ T2,
                                        const uint16_t* __restrict__ gw, const uint16_t* __restrict__ ea,
                                        const int* __restrict__ src, const int* __restrict__ dst,
                                        uint16_t* __restrict__ out, int N, int E, int s, int node0) {
    float* accs = (float*)smem;
    unsigned* queue = (unsigned*)(smem + NB * FS * 4);
    static_assert(NB * FS * 4 + 2048 <= K2_CNT, "slow-path LDS");
    const unsigned* t2g = T2 + ((size_t)s * N + node0) * FS;
    const int tid = threadIdx.x, f = tid & 63, w = tid >> 6;
    __syncthreads();
    for (int i = tid; i < NB * FS; i += 512) accs[i] = 0.f;
    const uint16_t* cf = gw + (size_t)2 * D * D + s * FS + (f & 32) + (f & 1) * 16 + ((f & 31) >> 1);
    const float c0 = bf_lo(cf[0]) * NLOG2E, c1 = bf_lo(cf[D]) * NLOG2E;
    const float c2 = bf_lo(cf[2 * D]) * NLOG2E, c3 = bf_lo(cf[3 * D]) * NLOG2E;
#pragma unroll 1
    for (int eb = 0; eb < E; eb += 512) {
        __syncthreads();
        const int e = eb + tid;
        const int dn = (e < E) ? dst[e] - node0 : -1;
        queue[tid] = (dn >= 0 && dn < NB) ? (unsigned)dn : 0xffffffffu;
        __syncthreads();
#pragma unroll 1
        for (int q0 = 0; q0 < 512; q0 += 64) {
            const unsigned qv = queue[q0 + f];
            unsigned long long m = __ballot(qv != 0xffffffffu && (qv & 7u) == (unsigned)w);
            while (m) {
                const int l = __builtin_ctzll(m);
                m &= m - 1;
                const int ee = eb + q0 + l;
                const int d2 = __builtin_amdgcn_readlane(qv, l);
                const unsigned tv = T1[((size_t)s * N + src[ee]) * FS + f];
                const u32x2 e2 = *(const u32x2*)(ea + (size_t)ee * 4);
                const float z = bf_lo(t2g[d2 * FS + f]) + bf_lo(tv) + bf_lo(e2.x) * c0 + bf_hi(e2.x) * c1
                                + bf_lo(e2.y) * c2 + bf_hi(e2.y) * c3;
                accs[d2 * FS + f] += __builtin_bit_cast(float, tv) * __builtin_amdgcn_rcpf(1.0f + __builtin_amdgcn_exp2f(z));
            }
        }
    }
    __syncthreads();
#pragma unroll 1
    for (int p = tid; p < NB * 16; p += 512) {
        const int r = p >> 4, q4 = (p & 15) * 4;
        if (node0 + r < N) {
            f32x4 y;
#pragma unroll
            for (int k = 0; k < 4; ++k) {
                const int sl = r * FS + (q4 & 32) + ((q4 & 15) + k) * 2 + ((q4 >> 4) & 1);
                y[k] = accs[sl] + bf_hi(t2g[sl]);
            }
            *(u32x2*)(out + (size_t)(node0 + r) * D + s * FS + q4) = silu_pack(y);
        }
    }
}

__global__ __launch_bounds__(512, 4)
void egc_k2(const unsigned* __restrict__ T1, const unsigned* __restrict__ T2,
            const unsigned* __restrict__ recs, const unsigned* __restrict__ tab,
            const uint16_t* __restrict__ gw, const uint16_t* __restrict__ ea,
            const int* __restrict__ src, const int* __restrict__ dst,
            uint16_t* __restrict__ out, int N, int E, int nchunks, int nbins, int ec, int force_slow)
{
    __shared__ __attribute__((aligned(16))) char smem[K2_LDS];
    unsigned* hist = (unsigned*)(smem + K2_HIST);
    unsigned* flag = (unsigned*)(smem + K2_FLAG);

    asm volatile("" :: "s"(T1), "s"(T2), "s"(recs), "s"(tab), "s"(gw), "s"(out), "s"(N), "s"(E), "s"(nchunks),
                       "s"(nbins), "s"(ec), "s"(force_slow));
    const int b   = blockIdx.x;
    const int s   = (b & 7) >> 1;
    const int hb  = (nbins + 1) >> 1;
    const int bin = (b & 1) * hb + (b >> 3);
    const int tid = threadIdx.x;
    const int lane = tid & 63;
    const int w = __builtin_amdgcn_readfirstlane(tid >> 6);
    const int g = lane >> 4, n = lane & 15;
    const int node0 = bin * NB;
    const unsigned lds0 = (unsigned)(size_t)(LDSP char*)smem;
    const rsrc_t t1r = mk_rsrc(T1 + (size_t)s * N * FS, (unsigned)N * 256u);
    __builtin_amdgcn_s_setprio(3);
    STAMP_DECL;
    STAMP(1, 0);

    const unsigned c = __umul24(tid, 5462u) >> 16, j = tid - c * TPC;
    unsigned te, cw0, cw1, cw2, cw3;
    asm volatile("buffer_load_dword %0, %5, %7, 0 offen\n\t"
                 "buffer_load_ushort %1, %6, %8, 0 offen\n\t"
                 "buffer_load_ushort %2, %6, %8, 0 offen offset:512\n\t"
                 "buffer_load_ushort %3, %6, %8, 0 offen offset:1024\n\t"
                 "buffer_load_ushort %4, %6, %8, 0 offen offset:1536"
                 : "=&v"(te), "=&v"(cw0), "=&v"(cw1), "=&v"(cw2), "=&v"(cw3)
                 : "v"(c * 4), "v"((n >> 3) * 64 + (n & 7) * 4 + (g >> 1) * 2 + (g & 1) * 32), "s"(mk_rsrc(tab + bin * TSTR, bin < nbins ? nchunks * 4 : 0)),
                   "s"(mk_rsrc(gw + (size_t)2 * D * D + s * FS, 2048 - s * 128)) : "memory");
    {
        const u32x4 sv = {SENT, SENT, SENT, SENT}, zv = {0u, 0u, 0u, 0u};
        u32x4* po = (u32x4*)(smem + K2_OFFS);
        constexpr int NO = 32 * LSTR / 4, NZ = (K2_PF - K2_EV) / 16;
        po[tid] = sv;
        if (tid < NO - 512) po[tid + 512] = sv;
        u32x4* pz = (u32x4*)(smem + K2_EV);
#pragma unroll
        for (int i = 0; i < NZ / 512; ++i) pz[tid + 512 * i] = zv;
        if (tid < NZ % 512) pz[tid + NZ / 512 * 512] = zv;
    }
    asm volatile("s_waitcnt vmcnt(0)" : "+v"(te), "+v"(cw0), "+v"(cw1), "+v"(cw2), "+v"(cw3) :: "memory");
    if (bin >= nbins) return;
    STAMP(1, 9);
    const int nc = (int)(te >> 16) - (int)j;
    u32x3 rec[SLOTS];
    unsigned long long vm[SLOTS];
#pragma unroll
    for (int i = 0; i < SLOTS; ++i) vm[i] = __builtin_amdgcn_ballot_w64(nc > TPC * i);
    {
        const rsrc_t rr = mk_rsrc(recs, (unsigned)E * 12u);
        const unsigned ro = (__umul24(c, ec) + (te & 0xffffu) + j) * 12u;
        asm volatile("buffer_load_dwordx3 %0, %5, %8, 0 offen\n\t"
                     "buffer_load_dwordx3 %1, %5, %8, 0 offen offset:144\n\t"
                     "buffer_load_dwordx3 %2, %5, %8, 0 offen offset:288\n\t"
                     "buffer_load_dwordx3 %3, %6, %8, 0 offen offset:432\n\t"
                     "buffer_load_dwordx3 %4, %7, %8, 0 offen offset:576"
                     : "=&v"(rec[0]), "=&v"(rec[1]), "=&v"(rec[2]), "=&v"(rec[3]), "=&v"(rec[4])
                     : "v"(ro), "v"(nc > TPC * 3 ? ro : OOB), "v"(nc > TPC * 4 ? ro : OOB), "s"(rr) : "memory");
    }
    const u32x2 cf = {pack_bf16(bf_lo(cw0) * NLOG2E, bf_lo(cw1) * NLOG2E), pack_bf16(bf_lo(cw2) * NLOG2E, bf_lo(cw3) * NLOG2E)};
    u32x4 asel;
#pragma unroll
    for (int k = 0; k < 4; ++k) asel[k] = (n == 4 * g + k) ? 0x3f80u : 0u;
    u32x2 lo16 = {(unsigned)n * 16u, (unsigned)n * 16u};
    unsigned vop = lds0 + K2_OFFS + (w * 4 + g) * (LSTR * 4);
    unsigned vep = lds0 + K2_EV + (((n & 3) == g) ? w * 4 + (n >> 2) : 32) * (LSTR * 8);
    u32x2 cfv = cf;
    asm volatile("" : "+v"(asel), "+v"(cfv), "+v"(lo16), "+v"(vop), "+v"(vep));
    __syncthreads();
    STAMP(1, 1);
    if (nc > TPC * SLOTS || force_slow) flag[0] = 1u;
    asm volatile("s_waitcnt vmcnt(0)" : "+v"(rec[0]), "+v"(rec[1]), "+v"(rec[2]), "+v"(rec[3]), "+v"(rec[4]) :: "memory");
    STAMP(1, 10);
    unsigned ca[SLOTS], rk[SLOTS];
#pragma unroll
    for (int i = 0; i < SLOTS; ++i) ca[i] = lds0 + K2_CNT + (w & 4) + (rec[i].z & 0xffffu);
#define K2_AT(R, A, M) "s_mov_b64 exec, %[" #M "]\n\tds_add_rtn_u32 %[" #R "], %[" #A "], %[one]\n\t"
    asm volatile(K2_AT(r0, a0, m0) K2_AT(r1, a1, m1) K2_AT(r2, a2, m2) K2_AT(r3, a3, m3) K2_AT(r4, a4, m4)
                 "s_mov_b64 exec, -1"
                 : [r0] "=&v"(rk[0]), [r1] "=&v"(rk[1]), [r2] "=&v"(rk[2]), [r3] "=&v"(rk[3]), [r4] "=&v"(rk[4])
                 : [a0] "v"(ca[0]), [a1] "v"(ca[1]), [a2] "v"(ca[2]), [a3] "v"(ca[3]), [a4] "v"(ca[4]),
                   [m0] "s"(vm[0]), [m1] "s"(vm[1]), [m2] "s"(vm[2]), [m3] "s"(vm[3]), [m4] "s"(vm[4]),
                   [one] "v"(1u << (8 * (w & 3)))
                 : "memory");
    asm volatile("s_waitcnt lgkmcnt(0)" : "+v"(rk[0]), "+v"(rk[1]), "+v"(rk[2]), "+v"(rk[3]), "+v"(rk[4]) :: "memory");
    __syncthreads();
    STAMP(1, 2);
    if (w >= 4) {
#pragma unroll
        for (int i = 0; i < 2 * EXP_PF; ++i)
            dma16(t1r, (b >> 3) * 4096u + (tid - 256) * 16u, hb * 4096u * i, lds0 + K2_PF + w * 1024);
    }

    unsigned pre[NBK], deg = 0, key = 0, rank = 0;
    if (tid < NB) {
        u32x2 f2[NBK];
#pragma unroll
        for (int k = 0; k < NBK; ++k) f2[k] = *(const u32x2*)(smem + K2_CNT + (k * NB + tid) * 16);
#pragma unroll
        for (int k = 0; k < NBK; ++k) {
            pre[k] = deg;
            deg = __builtin_amdgcn_sad_u8(f2[k][1], 0u, __builtin_amdgcn_sad_u8(f2[k][0], 0u, deg));
        }
        key = 63u - min(deg, 63u);
        rank = atomicAdd(&hist[key], 1u);
    }
    {
        unsigned long long bad = 0;
#pragma unroll
        for (int i = 0; i < SLOTS; ++i) {
            rk[i] = (rk[i] >> (8 * (w & 3))) & 255u;
            bad |= __builtin_amdgcn_ballot_w64(rk[i] > 63u) & vm[i];
        }
        if (bad) flag[0] = 1u;
    }
    __syncthreads();
    STAMP(1, 6);
    unsigned wv = 0, pos = 0;
    if (w < 2) {
        const unsigned hv = hist[lane];
        const unsigned ex = wave_scan(hv) - hv;
        rank += __builtin_amdgcn_ds_bpermute(key * 4, ex);
        pos = rank >> 4;
        wv = ((rank >> 2) & 3u) ^ ((pos & 1u) ? 3u : 0u);
        if (pos >= 2) { wv += 4; pos -= 2; }
        if (tid < NB) {
            smem[K2_ORD + wv * 16 + (rank & 3u) * 4 + pos] = (char)tid;
            if ((rank & 3u) == 0) smem[K2_LEN + wv * 4 + pos] = (char)min(max(deg, MINL), 255u);
        }
    }
    __syncthreads();
    STAMP(1, 7);
    if (tid < NB) {
        const unsigned l4 = *(const unsigned*)(smem + K2_LEN + wv * 4);
        const unsigned first = __builtin_amdgcn_sad_u8(l4 & ((1u << (8 * pos)) - 1u), 0u, 0u);
        const unsigned base = (wv * 4 + (rank & 3u)) * LSTR + first;
        if (first + max(deg, MINL) > MAXT) flag[0] = 1u;
#pragma unroll
        for (int k = 0; k < NBK; ++k) *(unsigned*)(smem + K2_CNT + (k * NB + tid) * 16 + 8) = base + pre[k];
    }
    unsigned lens = __builtin_amdgcn_readfirstlane(*(const unsigned*)(smem + K2_LEN + w * 4));
    unsigned ordb = *(const unsigned*)(smem + K2_ORD + w * 16 + g * 4);
    const rsrc_t t2r = mk_rsrc(T2 + ((size_t)s * N + node0) * FS, (unsigned)(N - node0) * 256u);
    __syncthreads();
    STAMP(1, 8);

    if (flag[0]) {
        WAIT_V(0);
        k2_slow(smem, T1, T2, gw, ea, src, dst, out, N, E, s, node0);
        return;
    }

    {
        const unsigned lowm = (1u << (8 * (w & 3))) - 1u;
        const unsigned m0 = (w & 4) ? 0xffffffffu : lowm, m1 = (w & 4) ? lowm : 0u;
        u32x4 cs[SLOTS];
#pragma unroll
        for (int i = 0; i < SLOTS; ++i) cs[i] = *(const u32x4*)(smem + K2_CNT + (rec[i].z & 0xffffu));
        __builtin_amdgcn_sched_barrier(0);
#pragma unroll
        for (int i = 0; i < SLOTS; ++i) {
            const unsigned p = __builtin_amdgcn_sad_u8(cs[i][1] & m1, 0u, __builtin_amdgcn_sad_u8(cs[i][0] & m0, 0u, rk[i])) + cs[i][2];
            const unsigned ov = (rec[i].z >> 16) << 8;
            asm volatile("s_mov_b64 exec, %4\n\tds_write_b32 %0, %1 offset:%c5\n\tds_write_b64 %2, %3 offset:%c6\n\ts_mov_b64 exec, -1"
                         :: "v"(p * 4), "v"(ov), "v"(p * 8), "v"(u32x2{rec[i].x, rec[i].y}), "s"(vm[i]), "i"(K2_OFFS), "i"(K2_EV) : "memory");
        }
    }
    const rsrc_t outr = mk_rsrc(out + (size_t)node0 * D + s * FS, (unsigned)(N - node0) * 512u - s * 128);
    WAIT_L0();
    __syncthreads();
    STAMP(1, 3);
    unsigned tot = (lens & 255) + ((lens >> 8) & 255) + ((lens >> 16) & 255);

    BLK_DECL;
#define K2_STEP(K, D0, D1, D2, D3, DN, Z0, Z1, Z2, Z3, ZN, EK, CF, AK, PRE, MID, POST)   \
        "s_sub_u32 s66, s66, 1\n\t"                                         \
        "s_cbranch_scc1 .Lsw" #K "_%=\n"                                          \
        ".Lrt" #K "_%=:\n\t"                                                      \
        "s_waitcnt lgkmcnt(2)\n\t"                                                \
        PRE                                                                       \
        AB_M("v_mfma_f32_16x16x32_bf16 " ZN ", " EK ", " CF ", v[18:21]\n\t")     \
        MID                                                                       \
        "v_exp_f32 v" #Z0 ", v" #Z0 "\n\t"                                        \
        "v_exp_f32 v" #Z1 ", v" #Z1 "\n\t"                                        \
        "v_exp_f32 v" #Z2 ", v" #Z2 "\n\t"                                        \
        "v_exp_f32 v" #Z3 ", v" #Z3 "\n\t"                                        \
        "v_pk_add_f32 v[" #Z0 ":" #Z1 "], v[" #Z0 ":" #Z1 "], 1.0 op_sel_hi:[1,0]\n\t" \
        "v_pk_add_f32 v[" #Z2 ":" #Z3 "], v[" #Z2 ":" #Z3 "], 1.0 op_sel_hi:[1,0]\n\t" \
        AB_G("s_waitcnt vmcnt(6)\n\t")                                            \
        AB_M("v_mfma_f32_16x16x32_bf16 " ZN ", v[8:11], " DN ", " ZN "\n\t")      \
        AB_T("v_rcp_f32 v" #Z0 ", v" #Z0 "\n\t")                                  \
        AB_T("v_rcp_f32 v" #Z1 ", v" #Z1 "\n\t")                                  \
        AB_T("v_rcp_f32 v" #Z2 ", v" #Z2 "\n\t")                                  \
        AB_T("v_rcp_f32 v" #Z3 ", v" #Z3 "\n\t")                                  \
        "v_pk_fma_f32 v[22:23], v[" #Z0 ":" #Z1 "], v[" #D0 ":" #D1 "], v[22:23]\n\t" \
        "v_pk_fma_f32 v[24:25], v[" #Z2 ":" #Z3 "], v[" #D2 ":" #D3 "], v[24:25]\n\t" \
        AB_G("buffer_load_dwordx4 v[" #D0 ":" #D3 "], " AK ", %[t1r], 0 offen\n\t") \
        POST
#define K2_SW(K) ".Lsw" #K "_%=:\n\ts_swappc_b64 s[62:63], s[60:61]\n\ts_branch .Lrt" #K "_%=\n"
#define K2_ADDA "v_pk_add_f32 v[46:47], v[46:47], v[50:51]\n\t"
#define K2_ADDB "v_pk_add_f32 v[48:49], v[48:49], v[50:51]\n\t"
#define K2_EA(O) AB_E("ds_read_b128 v[38:41], v53 offset:" #O "\n\t")
#define K2_EB(O) AB_E("ds_read_b128 v[42:45], v53 offset:" #O "\n\t")
#define K2_AA(O) "ds_read_b64 v[46:47], v52 offset:" #O "\n\t"
#define K2_AB(O) "ds_read_b64 v[48:49], v52 offset:" #O "\n\t"
#define K2_NEXT_T2                                                                \
        "v_and_b32 v57, 0xff, v54\n\t"                                            \
        "v_lshrrev_b32 v54, 8, v54\n\t"                                           \
        "v_lshl_add_u32 v58, v57, 8, v50\n\t"                                     \
        "v_lshl_add_u32 v59, v57, 9, v7\n\t"                                      \
        "buffer_load_dwordx4 v[26:29], v58, %[t2r], 0 offen\n\t"
#define K2_CIN  "v_lshlrev_b32 v18, 16, v26\n\tv_lshlrev_b32 v19, 16, v27\n\tv_lshlrev_b32 v20, 16, v28\n\tv_lshlrev_b32 v21, 16, v29\n\t"
#define K2_ACC  "v_and_b32 v22, 0xffff0000, v26\n\tv_and_b32 v23, 0xffff0000, v27\n\tv_and_b32 v24, 0xffff0000, v28\n\tv_and_b32 v25, 0xffff0000, v29\n\tv_mov_b32 v56, v59\n\t"
#define K2_LD(D0, D3, A) "buffer_load_dwordx4 v[" #D0 ":" #D3 "], v" #A ", %[t1r], 0 offen\n\t"
    asm volatile(
        "s_waitcnt lgkmcnt(0)\n\t"
        "ds_read_b128 v[30:33], v52\n\t"
        "ds_read_b128 v[34:37], v52 offset:16\n\t"
        K2_NEXT_T2
        K2_EA(0) K2_AA(32) K2_EB(16) K2_AB(40)
        "s_getpc_b64 s[60:61]\n"
        ".Lpc_%=:\n\t"
        "s_add_u32 s60, s60, .Lhnd_%=-.Lpc_%=\n\t"
        "s_addc_u32 s61, s61, 0\n\t"
        "s_and_b32 s66, s64, 0xff\n\t"
        "s_sub_u32 s66, s66, 1\n\t"
        "s_mov_b32 s67, 0\n\t"
        "s_mov_b32 s68, 0xbfb8aa3b\n\t"
        "v_mov_b32 v12, 0\n\t"
        "v_mov_b32 v13, 0\n\t"
        "v_mov_b32 v16, 0\n\t"
        "v_mov_b32 v17, 0\n\t"
        "s_waitcnt lgkmcnt(4)\n\t"
        "v_pk_add_f32 v[30:31], v[30:31], v[50:51]\n\t"
        "v_pk_add_f32 v[32:33], v[32:33], v[50:51]\n\t"
        "v_pk_add_f32 v[34:35], v[34:35], v[50:51]\n\t"
        "v_pk_add_f32 v[36:37], v[36:37], v[50:51]\n\t"
        K2_LD(64, 67, 30) K2_LD(68, 71, 31) K2_LD(72, 75, 32) K2_LD(76, 79, 33)
        K2_LD(80, 83, 34) K2_LD(84, 87, 35) K2_LD(88, 91, 36) K2_LD(92, 95, 37)
        "s_setprio 2\n\t"
        "s_waitcnt vmcnt(8) lgkmcnt(3)\n\t"
        K2_CIN K2_ACC
        K2_NEXT_T2
        "v_mfma_f32_16x16x32_bf16 v[30:33], v[38:41], v[14:17], v[18:21]\n\t"
        "s_waitcnt vmcnt(8)\n\t"
        "v_mfma_f32_16x16x32_bf16 v[30:33], v[8:11], v[64:67], v[30:33]\n\t"
        BLK_INIT
        "s_nop 3\n"
        ".Lloop_%=:\n\t"
        K2_STEP(0, 64, 65, 66, 67, "v[68:71]", 30, 31, 32, 33, "v[34:37]", "v[38:41]", "v[12:15]", "v46", K2_ADDA, K2_EA(32), "")
        K2_STEP(1, 68, 69, 70, 71, "v[72:75]", 34, 35, 36, 37, "v[30:33]", "v[42:45]", "v[14:17]", "v47", "", "", K2_AA(48))
        K2_STEP(2, 72, 73, 74, 75, "v[76:79]", 30, 31, 32, 33, "v[34:37]", "v[42:45]", "v[12:15]", "v48", K2_ADDB, K2_EB(48), "")
        K2_STEP(3, 76, 77, 78, 79, "v[80:83]", 34, 35, 36, 37, "v[30:33]", "v[38:41]", "v[14:17]", "v49", "", "", K2_AB(56))
        K2_STEP(4, 80, 81, 82, 83, "v[84:87]", 30, 31, 32, 33, "v[34:37]", "v[38:41]", "v[12:15]", "v46", K2_ADDA, K2_EA(64), "")
        K2_STEP(5, 84, 85, 86, 87, "v[88:91]", 34, 35, 36, 37, "v[30:33]", "v[42:45]", "v[14:17]", "v47", "", "", K2_AA(64))
        K2_STEP(6, 88, 89, 90, 91, "v[92:95]", 30, 31, 32, 33, "v[34:37]", "v[42:45]", "v[12:15]", "v48", K2_ADDB, K2_EB(80), "")
        K2_STEP(7, 92, 93, 94, 95, "v[64:67]", 34, 35, 36, 37, "v[30:33]", "v[38:41]", "v[14:17]", "v49", "", "", K2_AB(72))
        "v_add_u32 v53, 64, v53\n\t"
        "v_add_u32 v52, 32, v52\n\t"
        BLK_STAMP
        "s_sub_u32 s65, s65, 8\n\t"
        "s_cmp_gt_i32 s65, %c[p1]\n\t"
        "s_cbranch_scc1 .Lloop_%=\n\t"
        "s_setprio 1\n\t"
        "s_cmp_gt_i32 s65, %c[p2]\n\t"
        "s_cbranch_scc1 .Lloop_%=\n\t"
        "s_setprio 0\n\t"
        "s_branch .Lloop_%=\n"
        K2_SW(0) K2_SW(1) K2_SW(2) K2_SW(3) K2_SW(4) K2_SW(5) K2_SW(6) K2_SW(7)
        ".Lhnd_%=:\n\t"
        "s_cmp_lg_u32 s67, 0\n\t"
        "s_cbranch_scc1 .Lflush_%=\n\t"
        K2_CIN
        "s_mov_b32 s66, 0\n\t"
        "s_mov_b32 s67, 1\n\t"
        "s_setpc_b64 s[62:63]\n"
        ".Lflush_%=:\n\t"
        "v_pk_mul_f32 v[60:61], v[22:23], s[68:69] op_sel_hi:[1,0]\n\t"
        "v_pk_mul_f32 v[62:63], v[24:25], s[68:69] op_sel_hi:[1,0]\n\t"
        "v_exp_f32 v60, v60\n\t"
        "v_exp_f32 v61, v61\n\t"
        "v_exp_f32 v62, v62\n\t"
        "v_exp_f32 v63, v63\n\t"
        "v_pk_add_f32 v[60:61], v[60:61], 1.0 op_sel_hi:[1,0]\n\t"
        "v_pk_add_f32 v[62:63], v[62:63], 1.0 op_sel_hi:[1,0]\n\t"
        "v_rcp_f32 v60, v60\n\t"
        "v_rcp_f32 v61, v61\n\t"
        "v_rcp_f32 v62, v62\n\t"
        "v_rcp_f32 v63, v63\n\t"
        "v_pk_mul_f32 v[60:61], v[60:61], v[22:23]\n\t"
        "v_pk_mul_f32 v[62:63], v[62:63], v[24:25]\n\t"
        "v_cvt_pk_bf16_f32 v62, v60, v62\n\t"
        "v_cvt_pk_bf16_f32 v63, v61, v63\n\t"
        "s_mov_b32 vcc_lo, 0x55555555\n\t"
        "s_mov_b32 vcc_hi, 0x55555555\n\t"
        "v_cndmask_b32_dpp v60, v63, v62, vcc quad_perm:[1,0,3,2] row_mask:0xf bank_mask:0xf\n\t"
        "s_not_b64 vcc, vcc\n\t"
        "v_cndmask_b32_dpp v61, v62, v63, vcc quad_perm:[1,0,3,2] row_mask:0xf bank_mask:0xf\n\t"
        "s_lshr_b32 s64, s64, 8\n\t"
        "buffer_store_dwordx2 v[60:61], v56, %[outr], 0 offen\n\t"
        "s_and_b32 s66, s64, 0xff\n\t"
        "s_cbranch_scc0 .Ldone_%=\n\t"
        K2_ACC
        K2_NEXT_T2
        "s_sub_u32 s66, s66, 2\n\t"
        "s_mov_b32 s67, 0\n\t"
        "s_setpc_b64 s[62:63]\n"
        ".Ldone_%=:\n\t"
        "s_waitcnt vmcnt(0) lgkmcnt(0)\n\t"
        BLK_LAST
        : BLK_OUT "+{s64}"(lens), "+{s65}"(tot), "+{v52}"(vop), "+{v53}"(vep), "+{v54}"(ordb)
        : [p1] "i"(EXP_P1), [p2] "i"(EXP_P2), [t1r] "s"(t1r), [t2r] "s"(t2r), [outr] "s"(outr), "{v[8:11]}"(asel), "{v[14:15]}"(cfv), "{v[50:51]}"(lo16),
          "{v7}"((unsigned)((n >> 3) * 64 + (n & 6) * 4 + (n & 1) * 32))
        : "v12", "v13", "v16", "v17", "v18", "v19", "v20", "v21", "v22", "v23", "v24", "v25", "v26", "v27", "v28", "v29",
          "v30", "v31", "v32", "v33", "v34", "v35", "v36", "v37", "v38", "v39", "v40", "v41", "v42", "v43", "v44", "v45",
          "v46", "v47", "v48", "v49", "v56", "v57", "v58", "v59", "v60", "v61", "v62", "v63",
          "v64", "v65", "v66", "v67", "v68", "v69", "v70", "v71", "v72", "v73", "v74", "v75", "v76", "v77", "v78", "v79",
          "v80", "v81", "v82", "v83", "v84", "v85", "v86", "v87", "v88", "v89", "v90", "v91", "v92", "v93", "v94", "v95",
          "s60", "s61", "s62", "s63", "s66", "s67", "s68", "s69", "s70", "s71", "vcc", "scc", "memory");
    BLK_FLUSH;
    STAMP(1, 5);
    STAMP_FLUSH(1, 5);
    WSTAMP(0, stv[3]);
    WSTAMP(1, stv[5]);
    WSTAMP(2, tsteps);
    WSTAMP(3, (unsigned long long)__builtin_amdgcn_s_getreg(63492) | ((unsigned long long)__builtin_amdgcn_s_getreg(63508) << 32));
}

}

extern "C" void kernel_launch(void* const* d_in, const int* in_sizes, int n_in,
                              void* d_out, int out_size, void* d_ws, size_t ws_size,
                              hipStream_t stream) {
    const uint16_t* x  = (const uint16_t*)d_in[0];
    const uint16_t* ea = (const uint16_t*)d_in[1];
    const int* src     = (const int*)d_in[2];
    const int* dst     = (const int*)d_in[3];
    const uint16_t* uw = (const uint16_t*)d_in[4];
    const uint16_t* vw = (const uint16_t*)d_in[5];
    const uint16_t* gw = (const uint16_t*)d_in[6];
    const int N = in_sizes[0] / D;
    const int E = in_sizes[2];
    const int nrb   = (N + BM - 1) / BM;
    const int nbins = (N + NB - 1) / NB;
    int nchunks = 40;
    const int slots = nrb + nchunks / 8;
    const int ec = ((E + nchunks - 1) / nchunks + 3) & ~3;
    const int force_slow = (nbins > BINMAX || ec > RMAX * 2048) ? 1 : 0;
    if (force_slow) nchunks = 0;

    char* ws = (char*)d_ws;
    const size_t tb = ((size_t)NS * N * FS * 4 + 255) & ~(size_t)255;
    unsigned* T1 = (unsigned*)ws;
    unsigned* T2 = (unsigned*)(ws + tb);
    unsigned* recs = (unsigned*)(ws + 2 * tb);
    unsigned* tab = (unsigned*)(ws + 2 * tb + (((size_t)E * 12 + 255) & ~(size_t)255));

    (void)hipFuncSetAttribute((const void*)egc_k1, hipFuncAttributeMaxDynamicSharedMemorySize, K1_LDS);
    egc_k1<<<slots * 8, 512, K1_LDS, stream>>>(x, ea, src, dst, uw, vw, gw, T1, T2, recs, tab,
                                                 N, E, nrb, nbins, ec, nchunks, (N + nrb - 1) / nrb, NBK * 65536 / N);
    egc_k2<<<((nbins + 1) / 2) * 8, 512, 0, stream>>>(T1, T2, recs, tab, gw, ea, src, dst, (uint16_t*)d_out,
                                                      N, E, nchunks, nbins, ec, force_slow);
}
```

```cpp
#include <hip/hip_runtime.h>
#include <hip/hip_bf16.h>
#include <stdint.h>

typedef float    f32x4 __attribute__((ext_vector_type(4)));
typedef float    f32x2 __attribute__((ext_vector_type(2)));
typedef short    s16x4 __attribute__((ext_vector_type(4)));
typedef __bf16   bf16x8 __attribute__((ext_vector_type(8)));
typedef __bf16   bf16x2 __attribute__((ext_vector_type(2)));
typedef unsigned u32x4 __attribute__((ext_vector_type(4)));
typedef unsigned u32x3 __attribute__((ext_vector_type(3)));
typedef unsigned u32x2 __attribute__((ext_vector_type(2)));
typedef __amdgpu_buffer_rsrc_t rsrc_t;
#define LDSP __attribute__((address_space(3)))

#define BLK_DECL
#define BLK_INIT
#define BLK_STAMP
#define BLK_LAST
#define BLK_OUT
#define BLK_FLUSH
#define STAMP_DECL
#define STAMP_ARG
#define STAMP_PASS
#define STAMP_FLUSH(K, L) do {} while (0)
#define STAMP(K, I) do {} while (0)
#define WSTAMP(I, V) do {} while (0)

#define AB_M(x) x
#define AB_G(x) x
#define AB_E(x) x
#define AB_T(x) x
#define NBK 4
#define EXP_PF 2
#define EXP_K1A 0
#define EXP_BSL 40
#define EXP_WROT 1
#define EXP_NT 0
#define EXP_STG 1
#define EXP_RPF 0
#define EXP_SAFE 0
#define EXP_P1 20
#define EXP_P2 10
#define EXP_ROT 3
namespace {

constexpr int D     = 256;
constexpr int FS    = 64;
constexpr int NS    = 4;
constexpr int BM    = 384;
constexpr int NB    = 80;
constexpr int NBN   = 79;
constexpr int CMAX  = 42;
constexpr int TSTR  = 64;
constexpr int RMAX  = 2;
constexpr int BINMAX = 256;
constexpr int SLOTS = 8;
constexpr int TPC   = 12;
constexpr int LSTR  = 72;
constexpr int MAXT  = 58;
constexpr unsigned MINL = 8;
constexpr float NLOG2E = -1.4426950408889634f;
constexpr unsigned SENT = 0x00fff000u;
constexpr unsigned OOB  = 0x7ffffff0u;
constexpr int AUX_WT = 16;

__device__ __forceinline__ rsrc_t mk_rsrc(const void* p, unsigned bytes) {
    return __builtin_amdgcn_make_buffer_rsrc((void*)p, (short)0, (int)bytes, 0x00020000);
}
__device__ __forceinline__ void dma16(rsrc_t r, unsigned voff, unsigned soff, unsigned lds) {
    asm volatile("s_mov_b32 m0, %0\n\ts_nop 0\n\tbuffer_load_dwordx4 %1, %2, %3 offen lds"
                 :: "s"(lds), "v"(voff), "s"(r), "s"(soff) : "m0", "memory");
}
__device__ __forceinline__ void dma16nt(rsrc_t r, unsigned voff, unsigned soff, unsigned lds) {
    asm volatile("s_mov_b32 m0, %0\n\ts_nop 0\n\tbuffer_load_dwordx4 %1, %2, %3 offen nt lds"
                 :: "s"(lds), "v"(voff), "s"(r), "s"(soff) : "m0", "memory");
}
#define WAIT_V(n) asm volatile("s_waitcnt vmcnt(%0)" :: "i"(n) : "memory")
#define WAIT_L0() asm volatile("s_waitcnt lgkmcnt(0)" ::: "memory")
#define BAR() __builtin_amdgcn_s_barrier()

__device__ __forceinline__ unsigned wave_scan(unsigned v) {
    v += __builtin_amdgcn_update_dpp(0u, v, 0x111, 0xf, 0xf, false);
    v += __builtin_amdgcn_update_dpp(0u, v, 0x112, 0xf, 0xf, false);
    v += __builtin_amdgcn_update_dpp(0u, v, 0x114, 0xf, 0xf, false);
    v += __builtin_amdgcn_update_dpp(0u, v, 0x118, 0xf, 0xf, false);
    v += __builtin_amdgcn_update_dpp(0u, v, 0x142, 0xa, 0xf, false);
    v += __builtin_amdgcn_update_dpp(0u, v, 0x143, 0xc, 0xf, false);
    return v;
}
__device__ __forceinline__ unsigned pack_bf16(float lo, float hi) {
    f32x2 p = {lo, hi};
    return __builtin_bit_cast(unsigned, __builtin_convertvector(p, bf16x2));
}
__device__ __forceinline__ float bf_lo(unsigned v) { return __builtin_bit_cast(float, v << 16); }
__device__ __forceinline__ float bf_hi(unsigned v) { return __builtin_bit_cast(float, v & 0xffff0000u); }

__device__ __forceinline__ unsigned pack_t1(float xb, float hv) {
    asm("" : "+v"(xb));
    const float p = xb * NLOG2E;
    unsigned P;
    asm("v_cvt_pk_bf16_f32 %0, %1, -%1" : "=v"(P) : "v"(p));
    const unsigned t = __builtin_bit_cast(unsigned, hv) - (unsigned)((int)P >> 16);
    return (t & 0xffff0000u) | (P & 0xffffu);
}

constexpr int K1_W   = 98304;
constexpr int K1_LDS = 163840;
__host__ __device__ constexpr int K1_R(int r) { return r == 5 ? 0 : r * 32768; }

__device__ __forceinline__ void k1_gemm(char* smem, const unsigned lds0, const int lane, const int w, const unsigned g0,
                                        const unsigned xvo, const rsrc_t xr, const rsrc_t tr,
                                        const rsrc_t pr, const unsigned pl, const unsigned pstep, const unsigned ec, const unsigned ph, const unsigned peo STAMP_ARG) {
    const int g = lane >> 4, n = lane & 15;
    const int fh = w & 1, rg = w >> 1;
    const unsigned ldw = lds0 + w * 1024;
    unsigned gr[6];
#pragma unroll
    for (int r = 0; r < 6; ++r) gr[r] = g0 + r > 5 ? g0 + r - 6 : g0 + r;
#define K1_LDX(r, i) dma16(xr, xvo, gr[r] * 32768 + (i) * 8192, ldw + K1_R(r) + (i) * 8192)

    const f32x4 zero4 = {0.f, 0.f, 0.f, 0.f}, tiny4 = {1e-36f, 1e-36f, 1e-36f, 1e-36f};
    bf16x8 wf[8][4];
    bf16x8 xq[8];
    f32x4 acc[2][4];
    const unsigned fr = ((n >> 3) & 1) | ((g & 1) << 1);
    const char* wp0 = smem + K1_W + (8 * g + (n >> 2)) * 128 + (((2 * fh) ^ fr) << 5) + 8 * (n & 3);
    const char* wp1 = smem + K1_W + (8 * g + (n >> 2)) * 128 + (((2 * fh + 1) ^ fr) << 5) + 8 * (n & 3);
    unsigned xa[3][4];
#pragma unroll
    for (int k = 0; k < 4; ++k) {
        xa[0][k] = lds0 + n * 512 + rg * 8192 + (((4 * k + g) ^ n) << 4);
        xa[1][k] = xa[0][k] + 65536; xa[2][k] = xa[0][k] + 131072;
    }
    const unsigned ovo = g * 1024 + fh * 128 + n * 8 + rg * 4096;
#define K1_RDW(ks) do { _Pragma("unroll") for (int mf = 0; mf < 4; ++mf) {                                   \
        const char* wp = ((mf & 1) ? wp1 : wp0) + (ks) * 8192 + (mf >> 1) * 4096;                           \
        u32x2 l2 = __builtin_bit_cast(u32x2, __builtin_amdgcn_ds_read_tr16_b64_v4i16((LDSP s16x4*)wp));      \
        u32x2 h2 = __builtin_bit_cast(u32x2, __builtin_amdgcn_ds_read_tr16_b64_v4i16((LDSP s16x4*)(wp + 512))); \
        wf[ks][mf] = __builtin_bit_cast(bf16x8, u32x4{l2.x, l2.y, h2.x, h2.y}); } } while (0)
#define K1_RDX(r, ks) xq[ks] = *(const LDSP bf16x8*)(size_t)(xa[K1_R(r) >> 16][(ks) & 3] + (K1_R(r) & 65535) + ((ks) >> 2) * 256)
#define K1_MM(p, ks) do { _Pragma("unroll") for (int m = 0; m < 4; ++m)                                      \
        acc[p][m] = __builtin_amdgcn_mfma_f32_16x16x32_bf16(xq[ks], wf[ks][m], (ks) ? acc[p][m] : (m < 2 ? zero4 : tiny4), 0, 0, 0); } while (0)
#define SB() __builtin_amdgcn_sched_barrier(0)

    WAIT_V(0); BAR();
    STAMP(0, 2);
    K1_RDW(0); K1_RDX(0, 0); K1_RDW(1); K1_RDX(0, 1);
    SB();
#pragma unroll
    for (int ks = 0; ks < 8; ++ks) {
        if (ks < 6) { K1_RDW(ks + 2); K1_RDX(0, ks + 2); }
        if (ks < 4) { K1_LDX(1 + (ks >> 1), (ks & 1) * 2); K1_LDX(1 + (ks >> 1), (ks & 1) * 2 + 1); }
        if (ks == 6) {
            WAIT_L0(); WAIT_V(0); BAR();
            STAMP(0, 3);
#pragma unroll
            for (int k = 0; k < 6; ++k) K1_RDX(1, k);
        }
        K1_MM(0, ks);
        if (ks >= 6) K1_RDX(1, ks);
        SB();
    }
#pragma unroll
    for (int r = 1; r < 6; ++r) {
        const int p = r & 1, q = p ^ 1;
        if (r == 1) STAMP(0, 4);
        if (r == 2) { STAMP(0, 7); WAIT_V(6); BAR(); STAMP(0, 8); }
        if (r == 3) { WAIT_V(8); BAR(); STAMP(0, 9); }
        if (r == 4) { WAIT_V(6); BAR(); STAMP(0, 11); }
        if (r == 5) STAMP(0, 12);
        const unsigned vo = ovo + gr[r - 1] * 16384;
        u32x2 o;
#pragma unroll
        for (int ks = 0; ks < 8; ++ks) {
            K1_MM(p, ks);
            if (r < 5) K1_RDX(r + 1, ks);
            o[ks & 1] = pack_t1(acc[q][ks & 1][ks >> 1], acc[q][2 + (ks & 1)][ks >> 1]);
            if (ks & 1) __builtin_amdgcn_raw_buffer_store_b64(o, tr, vo + (ks >> 1) * 256, 0, AUX_WT);
            if (r == 1) K1_LDX(3 + (ks >> 2), ks & 3);
            if (r == 2 && ks < 4) K1_LDX(5, ks);
            if (EXP_RPF && r == EXP_RPF && ks < 5) {
                const unsigned U = pl + pstep * (ks < 2 ? ks : ks - 2);
                const unsigned o = ks < 2 ? __umul24(U >> 9, ec * 4) + ph * 2 + (U & 511u) * 16u
                                          : __umul24(U >> 10, ec * 8) + ph * 4 + (U & 1023u) * 16u + peo;
                dma16(pr, o, 0, ldw + K1_R(1));
            }
#pragma unroll
            for (int i = 0; i < 4; ++i) {
                __builtin_amdgcn_sched_group_barrier(0x008, 1, 0);
                __builtin_amdgcn_sched_group_barrier(0x002, 1, 0);
            }
            SB();
        }
    }
    STAMP(0, 13);
#pragma unroll
    for (int r = 0; r < 4; ++r) {
        const u32x2 o = {pack_t1(acc[1][0][r], acc[1][2][r]), pack_t1(acc[1][1][r], acc[1][3][r])};
        __builtin_amdgcn_raw_buffer_store_b64(o, tr, ovo + gr[5] * 16384 + r * 256, 0, AUX_WT);
    }
#undef K1_LDX
}

__global__ __launch_bounds__(512, 2)
void egc_k1(const uint16_t* __restrict__ x, const uint16_t* __restrict__ ea,
            const int* __restrict__ src, const int* __restrict__ dst,
            const uint16_t* __restrict__ uw, const uint16_t* __restrict__ vw,
            const uint16_t* __restrict__ gw,
            unsigned* __restrict__ T1, unsigned* __restrict__ T2,
            unsigned* __restrict__ recs, unsigned* __restrict__ tab,
            int N, int E, int nrb, int nbins, int ec, int nchunks, int rows, int bmul)
{
    extern __shared__ __attribute__((aligned(16))) char smem[];
    const int b   = blockIdx.x;
    const int slot = b >> 3;
    const int item = (b & 7) * nrb + slot;
    const int rb  = item >> 3;
    const int ct  = item & 7;
    const int tid = threadIdx.x;
    const int lane = tid & 63;
    const int w = __builtin_amdgcn_readfirstlane(tid >> 6);
    asm volatile(".Legc_code_begin:" :: "s"(x), "s"(ea), "s"(src), "s"(dst), "s"(uw), "s"(vw), "s"(gw), "s"(T1), "s"(T2), "s"(recs), "s"(tab),
                       "s"(N), "s"(E), "s"(nrb), "s"(nbins), "s"(ec), "s"(nchunks), "s"(rows), "s"(bmul));
    STAMP_DECL;
    STAMP(0, 0);

    const int s = ct >> 1;
    const int row0 = rb * rows;
    const unsigned nr = (unsigned)max(min(rows, N - row0), 0);
    const unsigned lds0 = (unsigned)(size_t)(LDSP char*)smem;
    const rsrc_t xr = mk_rsrc(x + (size_t)row0 * D, nr * 512u);
    const unsigned rl = 2 * w + (lane >> 5);
    const unsigned xvo = rl * 512 + (((lane & 31) ^ rl) << 4);
    const unsigned g0 = ct >= 6 ? ct - 6 : ct;
    {
        const uint16_t* wsel = (ct & 1) ? (w < 4 ? gw : uw) : (w < 4 ? gw + D * D : vw);
        const unsigned f = ((lane >> 4) & 1) | ((w & 1) << 1);
        const unsigned wvo = (lane >> 3) * 512 + s * 128 + ((((lane >> 1) & 3) ^ f) << 5) + (lane & 1) * 16 + (w & 3) * 4096;
        unsigned t0, t1;
#define K1_W1 "s_lshl_b32 %1, %0, 1\n\ts_add_u32 m0, %0, %[lw]\n\ts_add_u32 %0, %0, 0x2000\n\t"                  \
              "buffer_load_dwordx4 %[wvo], %[wr], %1 offen lds\n\ts_and_b32 %0, %0, 0xe000\n\t"
#define K1_X1(O) "s_add_u32 m0, %[lx], " #O "\n\ts_add_u32 %1, %[gx], " #O "\n\tbuffer_load_dwordx4 %[xvo], %[xr], %1 offen lds\n\t"
        asm volatile("s_cmp_ge_i32 %[slot], %[nrb]\n\t"
                     "s_cbranch_scc1 .Lk1skip_%=\n\t"
                     "s_mov_b32 %0, %[k0]\n\t"
                     K1_W1 K1_W1 K1_W1 K1_W1 K1_W1 K1_W1 K1_W1 K1_W1
                     K1_X1(0) K1_X1(0x2000) K1_X1(0x4000) K1_X1(0x6000)
                     ".Lk1skip_%=:"
                     : "=&s"(t0), "=&s"(t1)
                     : [slot] "s"(slot), [nrb] "s"(nrb), [k0] "s"((rb & 3) * 16384), [lw] "s"(lds0 + w * 1024 + K1_W),
                       [wvo] "v"(wvo), [wr] "s"(mk_rsrc(wsel, D * D * 2)), [lx] "s"(lds0 + w * 1024), [gx] "s"(g0 * 32768),
                       [xvo] "v"(xvo), [xr] "s"(xr)
                     : "m0", "scc", "memory");
    }
    STAMP(0, 1);
    if (slot < nrb) {
        k1_gemm(smem, lds0, lane, w, g0, xvo, xr,
                mk_rsrc(((ct & 1) ? T2 : T1) + ((size_t)s * N + row0) * FS, nr * 256u),
                mk_rsrc(recs, nchunks ? (unsigned)E * 12u : 0u), slot * 512 + tid, nrb * 512, ec, (b & 1) * ec, ((E + 3) & ~3) * 4 STAMP_PASS);
        STAMP(0, 5);
        STAMP(0, 6);
        STAMP_FLUSH(0, 6);
        return;
    }
    {
        const int c = (slot - nrb) * 8 + (b & 7);
        unsigned cpf;
        {
            unsigned long long pc;
            unsigned len;
            unsigned back;
            asm volatile("s_getpc_b64 %0\n.Legc_code_here:\n\ts_mov_b32 %1, .Legc_code_end-.Legc_code_begin\n\t"
                         "s_mov_b32 %2, .Legc_code_here-.Legc_code_begin" : "=s"(pc), "=s"(len), "=s"(back));
            pc -= back;
            if (back > 65536u) len = 0;
            cpf = __builtin_amdgcn_raw_buffer_load_b32(mk_rsrc((const void*)pc, len > 65536u ? 0u : len), tid * 64, 0, 0);
        }
        if (c == 0 && tid < 256) {
            const int g = lane >> 4, n = lane & 15;
            const uint16_t* cw = gw + 2 * D * D + w * FS + (n >> 3) * 32 + (n & 7) * 2 + (g >> 1) + (g & 1) * 16;
            const u32x2 cf = {pack_bf16(bf_lo(cw[0]) * NLOG2E, bf_lo(cw[D]) * NLOG2E),
                              pack_bf16(bf_lo(cw[2 * D]) * NLOG2E, bf_lo(cw[3 * D]) * NLOG2E)};
            __builtin_amdgcn_raw_buffer_store_b64(cf, mk_rsrc(tab + BINMAX * TSTR, 2048), tid * 8, 0, AUX_WT);
        }
        if (c >= nchunks) { asm volatile("" :: "v"(cpf)); return; }
        unsigned* cw  = (unsigned*)smem;
        unsigned* wt  = cw + 8 * BINMAX;
        unsigned* rme = wt + 16;
        u32x2*    rev = (u32x2*)(rme + RMAX * 2048);
        const int e0 = c * ec;
        const int ne = max(min(E - e0, ec), 0);
        const rsrc_t dr = mk_rsrc(dst + e0, ne * 4), sr = mk_rsrc(src + e0, ne * 4), er = mk_rsrc(ea + (size_t)e0 * 4, ne * 8);
        u32x4 dv[RMAX], sv[RMAX], ev[RMAX][2];
#pragma unroll
        for (int r = 0; r < RMAX; ++r) dv[r] = __builtin_amdgcn_raw_buffer_load_b128(dr, tid * 16, r * 8192, 2);
        ((u32x4*)cw)[tid] = u32x4{0u, 0u, 0u, 0u};
        __builtin_amdgcn_sched_barrier(0);
        __builtin_amdgcn_s_sleep(EXP_BSL);
        __builtin_amdgcn_sched_barrier(0);
#pragma unroll
        for (int r = 0; r < RMAX; ++r) {
            sv[r] = __builtin_amdgcn_raw_buffer_load_b128(sr, tid * 16, r * 8192, 2);
            ev[r][0] = __builtin_amdgcn_raw_buffer_load_b128(er, tid * 32, r * 16384, 2);
            ev[r][1] = __builtin_amdgcn_raw_buffer_load_b128(er, tid * 32 + 16, r * 16384, 2);
        }
        __builtin_amdgcn_sched_barrier(0);
        __syncthreads();
        asm volatile("" :: "v"(cpf));
        unsigned* cmine = cw + w * BINMAX;
        unsigned rk[RMAX][4], bn[RMAX][4];
#pragma unroll
        for (int r = 0; r < RMAX; ++r)
#pragma unroll
            for (int k = 0; k < 4; ++k) {
                bn[r][k] = __umul24(dv[r][k], 53093u) >> 22;
                rk[r][k] = 0;
                if (r * 2048 + tid * 4 + k < ne) rk[r][k] = atomicAdd(&cmine[bn[r][k]], 1u);
            }
        __syncthreads();
        STAMP(2, 1);
        {
            unsigned pre[8], t = 0, inc = 0;
            if (tid < BINMAX) {
#pragma unroll
                for (int k = 0; k < 8; ++k) { pre[k] = t; t += cw[k * BINMAX + tid]; }
                inc = wave_scan(t);
                if (lane == 63) wt[w] = inc;
            }
            __syncthreads();
            if (tid < BINMAX) {
                const u32x4 w4 = *(const u32x4*)wt;
                const unsigned st = inc - t + (w > 0 ? w4[0] : 0u) + (w > 1 ? w4[1] : 0u) + (w > 2 ? w4[2] : 0u);
#pragma unroll
                for (int k = 0; k < 8; ++k) cw[k * BINMAX + tid] = st + pre[k];
                if (tid < nbins) __builtin_amdgcn_raw_buffer_store_b32(st | (t << 16), mk_rsrc(tab, BINMAX * TSTR * 4), (tid * TSTR + c) * 4, 0, AUX_WT);
            }
        }
        __syncthreads();
        STAMP(2, 2);
#pragma unroll
        for (int r = 0; r < RMAX; ++r)
#pragma unroll
            for (int k = 0; k < 4; ++k) rk[r][k] += cmine[bn[r][k]];
#pragma unroll
        for (int r = 0; r < RMAX; ++r)
#pragma unroll
            for (int k = 0; k < 4; ++k)
                if (r * 2048 + tid * 4 + k < ne) {
                    rev[rk[r][k]] = u32x2{ev[r][k >> 1][(k & 1) * 2], ev[r][k >> 1][(k & 1) * 2 + 1]};
                    rme[rk[r][k]] = (sv[r][k] << 16) + (dv[r][k] - bn[r][k] * NBN + (__umul24(sv[r][k], bmul) >> 16) * NB) * 16u;
                }
        __syncthreads();
        STAMP(2, 3);
        const rsrc_t omr = mk_rsrc(recs + e0, ne * 4), oer = mk_rsrc(recs + (size_t)((E + 3) & ~3) + (size_t)e0 * 2, ne * 8);
#pragma unroll
        for (int i = 0; i < RMAX * 3; ++i) {
            const u32x4 v = ((const u32x4*)rme)[tid + 512 * i];
            __builtin_amdgcn_raw_buffer_store_b128(v, i < RMAX ? omr : oer, tid * 16 + (i < RMAX ? i : i - RMAX) * 8192, 0, AUX_WT);
        }
        STAMP(2, 5);
        STAMP(2, 6);
        STAMP_FLUSH(2, 6);
    }
}

constexpr int K2_OFFS = 0;
constexpr int K2_EV   = K2_OFFS + 32 * LSTR * 4;
constexpr int K2_CNT  = K2_EV + 33 * LSTR * 8;
constexpr int K2_HIST = K2_CNT + NBK * NB * 16;
constexpr int K2_ORD  = K2_HIST + 256;
constexpr int K2_LEN  = K2_ORD + 128;
constexpr int K2_FLAG = K2_LEN + 32;
constexpr int K2_PF   = K2_FLAG + 16;
constexpr int K2_LDS  = K2_PF + 8192;
static_assert((K2_PF - K2_EV) % 16 == 0 && K2_EV % 16 == 0, "LDS map");

__device__ __forceinline__ u32x2 silu_pack(f32x4 y) {
    f32x4 o;
#pragma unroll
    for (int r = 0; r < 4; ++r) {
        float t = __builtin_amdgcn_exp2f(y[r] * NLOG2E);
        o[r] = y[r] * __builtin_amdgcn_rcpf(1.0f + t);
    }
    return u32x2{pack_bf16(o[0], o[1]), pack_bf16(o[2], o[3])};
}

__device__ __forceinline__ void k2_slow(char* smem, const unsigned* __restrict__ T1, const unsigned* __restrict__ T2,
                                        const uint16_t* __restrict__ gw, const uint16_t* __restrict__ ea,
                                        const int* __restrict__ src, const int* __restrict__ dst,
                                        uint16_t* __restrict__ out, int N, int E, int s, int node0) {
    float* accs = (float*)smem;
    unsigned* queue = (unsigned*)(smem + NB * FS * 4);
    static_assert(NB * FS * 4 + 2048 <= K2_CNT, "slow-path LDS");
    const unsigned* t2g = T2 + ((size_t)s * N + node0) * FS;
    const int tid = threadIdx.x, f = tid & 63, w = tid >> 6;
    __syncthreads();
    for (int i = tid; i < NB * FS; i += 512) accs[i] = 0.f;
    const uint16_t* cf = gw + (size_t)2 * D * D + s * FS + (f & 32) + (f & 1) * 16 + ((f & 31) >> 1);
    const float c0 = bf_lo(cf[0]) * NLOG2E, c1 = bf_lo(cf[D]) * NLOG2E;
    const float c2 = bf_lo(cf[2 * D]) * NLOG2E, c3 = bf_lo(cf[3 * D]) * NLOG2E;
#pragma unroll 1
    for (int eb = 0; eb < E; eb += 512) {
        __syncthreads();
        const int e = eb + tid;
        const int dn = (e < E) ? dst[e] - node0 : -1;
        queue[tid] = (dn >= 0 && dn < NBN) ? (unsigned)dn : 0xffffffffu;
        __syncthreads();
#pragma unroll 1
        for (int q0 = 0; q0 < 512; q0 += 64) {
            const unsigned qv = queue[q0 + f];
            unsigned long long m = __ballot(qv != 0xffffffffu && (qv & 7u) == (unsigned)w);
            while (m) {
                const int l = __builtin_ctzll(m);
                m &= m - 1;
                const int ee = eb + q0 + l;
                const int d2 = __builtin_amdgcn_readlane(qv, l);
                const unsigned tv = T1[((size_t)s * N + src[ee]) * FS + f];
                const u32x2 e2 = *(const u32x2*)(ea + (size_t)ee * 4);
                const float z = bf_lo(t2g[d2 * FS + f]) + bf_lo(tv) + bf_lo(e2.x) * c0 + bf_hi(e2.x) * c1
                                + bf_lo(e2.y) * c2 + bf_hi(e2.y) * c3;
                accs[d2 * FS + f] += __builtin_bit_cast(float, tv) * __builtin_amdgcn_rcpf(1.0f + __builtin_amdgcn_exp2f(z));
            }
        }
    }
    __syncthreads();
#pragma unroll 1
    for (int p = tid; p < NBN * 16; p += 512) {
        const int r = p >> 4, q4 = (p & 15) * 4;
        if (node0 + r < N) {
            f32x4 y;
#pragma unroll
            for (int k = 0; k < 4; ++k) {
                const int sl = r * FS + (q4 & 32) + ((q4 & 15) + k) * 2 + ((q4 >> 4) & 1);
                y[k] = accs[sl] + __builtin_bit_cast(float, t2g[sl]);
            }
            *(u32x2*)(out + (size_t)(node0 + r) * D + s * FS + q4) = silu_pack(y);
        }
    }
}

__global__ __launch_bounds__(512, 4)
void egc_k2(const unsigned* __restrict__ T1, const unsigned* __restrict__ T2,
            const unsigned* __restrict__ recs, const unsigned* __restrict__ tab,
            const uint16_t* __restrict__ gw, const uint16_t* __restrict__ ea,
            const int* __restrict__ src, const int* __restrict__ dst,
            uint16_t* __restrict__ out, int N, int E, int nchunks, int nbins, int ec, int force_slow)
{
    __shared__ __attribute__((aligned(16))) char smem[K2_LDS];
    unsigned* hist = (unsigned*)(smem + K2_HIST);
    unsigned* flag = (unsigned*)(smem + K2_FLAG);

    asm volatile("" :: "s"(T1), "s"(T2), "s"(recs), "s"(tab), "s"(out), "s"(N), "s"(E), "s"(nchunks),
                       "s"(nbins), "s"(ec), "s"(force_slow));
    const int b   = blockIdx.x;
    const int s   = (b & 7) >> 1;
    const int hb  = (nbins + 1) >> 1;
    const int bin = (b & 1) * hb + (b >> 3);
    const int tid = threadIdx.x;
    const int lane = tid & 63;
    const int w = __builtin_amdgcn_readfirstlane(tid >> 6);
    const int g = lane >> 4, n = lane & 15;
    const int node0 = bin * NBN;
    const unsigned lds0 = (unsigned)(size_t)(LDSP char*)smem;
    const rsrc_t t1r = mk_rsrc(T1 + (size_t)s * N * FS, (unsigned)N * 256u);
    __builtin_amdgcn_s_setprio(3);
    STAMP_DECL;
    STAMP(1, 0);

    const unsigned c = __umul24(tid, 5462u) >> 16, j = tid - c * TPC;
    unsigned te;
    asm volatile("buffer_load_dword %0, %1, %2, 0 offen"
                 : "=&v"(te) : "v"(c * 4), "s"(mk_rsrc(tab + bin * TSTR, bin < nbins ? nchunks * 4 : 0)) : "memory");
    {
        const u32x4 sv = {SENT, SENT, SENT, SENT}, zv = {0u, 0u, 0u, 0u};
        u32x4* po = (u32x4*)(smem + K2_OFFS);
        constexpr int NO = 32 * LSTR / 4, NZ = (K2_PF - K2_EV) / 16;
        po[tid] = sv;
        if (tid < NO - 512) po[tid + 512] = sv;
        u32x4* pz = (u32x4*)(smem + K2_EV);
#pragma unroll
        for (int i = 0; i < NZ / 512; ++i) pz[tid + 512 * i] = zv;
        if (tid < NZ % 512) pz[tid + NZ / 512 * 512] = zv;
    }
    u32x4 asel;
#pragma unroll
    for (int k = 0; k < 4; ++k) asel[k] = (n == 4 * g + k) ? 0x3f80u : 0u;
    u32x2 lo16 = {(unsigned)n * 16u, (unsigned)n * 16u};
    unsigned vop = lds0 + K2_OFFS + (w * 4 + g) * (LSTR * 4);
    unsigned vep = lds0 + K2_EV + (((n & 3) == g) ? w * 4 + (n >> 2) : 32) * (LSTR * 8);
    unsigned olane = (n >> 3) * 64 + (n & 6) * 4 + (n & 1) * 32;
    unsigned j4 = 4 * j, rb0 = __umul24(c, ec) + j4;
    const rsrc_t mr = mk_rsrc(recs, (unsigned)E * 4u), er = mk_rsrc(recs + ((E + 3) & ~3), (unsigned)E * 8u);
    asm volatile("s_waitcnt vmcnt(0)" : "+v"(te), "+v"(asel), "+v"(lo16), "+v"(vop), "+v"(vep), "+v"(olane), "+v"(j4), "+v"(rb0)
                 : "s"(mr), "s"(er) : "memory");
    STAMP(1, 9);
    const int nc = (int)(te >> 16) - (int)j4;
    u32x4 rm[2], re[4];
    const unsigned r0 = nc > 0 ? (rb0 + (te & 0xffffu)) * 4u : OOB / 2;
    asm volatile("buffer_load_dwordx4 %0, %1, %2, 0 offen" : "=&v"(rm[0]) : "v"(r0), "s"(mr) : "memory");
    __builtin_amdgcn_sched_barrier(0);
    unsigned long long vm[SLOTS];
#pragma unroll
    for (int i = 0; i < SLOTS; ++i) vm[i] = __builtin_amdgcn_ballot_w64(nc > (i >> 2) * (4 * TPC) + (i & 3));
    const bool big = vm[4] != 0;
    asm volatile("s_cmp_eq_u64 %7, 0\n\t"
                 "s_cbranch_scc1 .Lnb_%=\n\t"
                 "buffer_load_dwordx4 %0, %3, %5, 0 offen offset:192\n\t"
                 "buffer_load_dwordx4 %1, %4, %6, 0 offen offset:384\n\t"
                 "buffer_load_dwordx4 %2, %4, %6, 0 offen offset:400\n\t"
                 "s_waitcnt vmcnt(0)\n"
                 ".Lnb_%=:"
                 : "=&v"(rm[1]), "=&v"(re[2]), "=&v"(re[3]) : "v"(r0), "v"(r0 * 2), "s"(mr), "s"(er), "s"(vm[4]) : "scc", "memory");
    if (bin >= nbins) { WAIT_V(0); return; }
    __syncthreads();
    STAMP(1, 1);
    if (nc > TPC * SLOTS || force_slow) flag[0] = 1u;
    asm volatile("s_waitcnt vmcnt(0)" : "+v"(rm[0]) :: "memory");
    STAMP(1, 10);
    u32x2 cfv;
    asm volatile("buffer_load_dwordx4 %0, %3, %5, 0 offen\n\t"
                 "buffer_load_dwordx4 %1, %3, %5, 0 offen offset:16\n\t"
                 "buffer_load_dwordx2 %2, %4, %6, 0 offen"
                 : "=&v"(re[0]), "=&v"(re[1]), "=&v"(cfv)
                 : "v"(r0 * 2), "v"(lane * 8), "s"(er), "s"(mk_rsrc(tab + BINMAX * TSTR + s * 128, 512)) : "memory");
    unsigned ca[SLOTS], rk[SLOTS];
#define K2_CA(I) (lds0 + K2_CNT + (w & 4) * 2 + (rm[(I) >> 2][(I) & 3] & 0xffffu))
#pragma unroll
    for (int i = 0; i < 4; ++i) ca[i] = K2_CA(i);
#define K2_AT(R, A, M) "s_mov_b64 exec, %[" #M "]\n\tds_add_rtn_u32 %[" #R "], %[" #A "], %[one]\n\t"
#define K2_AT4(O) asm volatile(K2_AT(r0, a0, m0) K2_AT(r1, a1, m1) K2_AT(r2, a2, m2) K2_AT(r3, a3, m3)                   \
                 "s_mov_b64 exec, -1\n\ts_waitcnt lgkmcnt(0)"                                                            \
                 : [r0] "=&v"(rk[O]), [r1] "=&v"(rk[O + 1]), [r2] "=&v"(rk[O + 2]), [r3] "=&v"(rk[O + 3])                \
                 : [a0] "v"(ca[O]), [a1] "v"(ca[O + 1]), [a2] "v"(ca[O + 2]), [a3] "v"(ca[O + 3]),                       \
                   [m0] "s"(vm[O]), [m1] "s"(vm[O + 1]), [m2] "s"(vm[O + 2]), [m3] "s"(vm[O + 3]),                       \
                   [one] "v"(1u << (8 * (w & 3))) : "memory")
    K2_AT4(0);
    asm volatile("" : "=v"(rk[4]), "=v"(rk[5]), "=v"(rk[6]), "=v"(rk[7]));
    if (big) {
#pragma unroll
        for (int i = 4; i < SLOTS; ++i) ca[i] = K2_CA(i);
        K2_AT4(4);
    }
    __syncthreads();
    STAMP(1, 2);
    if (w >= 4) {
#pragma unroll
        for (int i = 0; i < 2 * EXP_PF; ++i)
            dma16(t1r, (b >> 3) * 4096u + (tid - 256) * 16u, hb * 4096u * i, lds0 + K2_PF + w * 1024);
    }

    unsigned pre[2 * NBK], deg = 0, key = 0, rank = 0;
    if (tid < NB) {
        u32x4 f4[NBK];
#pragma unroll
        for (int k = 0; k < NBK; ++k) f4[k] = *(const u32x4*)(smem + K2_CNT + (k * NB + tid) * 16);
#pragma unroll
        for (int k = 0; k < NBK; ++k) {
            pre[2 * k] = deg;
            pre[2 * k + 1] = __builtin_amdgcn_sad_u8(f4[k][0], 0u, deg);
            deg = __builtin_amdgcn_sad_u8(f4[k][2], 0u, pre[2 * k + 1]);
        }
        key = 63u - min(deg, 63u);
        rank = atomicAdd(&hist[key], 1u);
    }
    {
        unsigned long long bad = 0;
#pragma unroll
        for (int i = 0; i < 4; ++i) {
            rk[i] = (rk[i] >> (8 * (w & 3))) & 255u;
            bad |= __builtin_amdgcn_ballot_w64(rk[i] > 63u) & vm[i];
        }
        if (big) {
#pragma unroll
            for (int i = 4; i < SLOTS; ++i) {
                rk[i] = (rk[i] >> (8 * (w & 3))) & 255u;
                bad |= __builtin_amdgcn_ballot_w64(rk[i] > 63u) & vm[i];
            }
        }
        if (bad) flag[0] = 1u;
    }
    __syncthreads();
    STAMP(1, 6);
    unsigned wv = 0, pos = 0;
    if (w < 2) {
        const unsigned hv = hist[lane];
        const unsigned ex = wave_scan(hv) - hv;
        rank += __builtin_amdgcn_ds_bpermute(key * 4, ex);
        pos = rank >> 4;
        wv = ((rank >> 2) & 3u) ^ (pos ? 3u : 0u);
        if (pos >= 2) { wv += 4; pos -= 2; }
        if (tid < NB) {
            smem[K2_ORD + wv * 16 + (rank & 3u) * 4 + pos] = (char)tid;
            if ((rank & 3u) == 0) smem[K2_LEN + wv * 4 + pos] = (char)min(max((deg + 1u) & ~1u, MINL), 254u);
        }
    }
    __syncthreads();
    STAMP(1, 7);
    if (tid < NB) {
        const unsigned l4 = *(const unsigned*)(smem + K2_LEN + wv * 4);
        const unsigned first = __builtin_amdgcn_sad_u8(l4 & ((1u << (8 * pos)) - 1u), 0u, 0u);
        const unsigned base = (wv * 4 + (rank & 3u)) * LSTR + first;
        if (first + max(deg, MINL) > MAXT) flag[0] = 1u;
#pragma unroll
        for (int k = 0; k < NBK; ++k) {
            *(unsigned*)(smem + K2_CNT + (k * NB + tid) * 16 + 4) = base + pre[2 * k];
            *(unsigned*)(smem + K2_CNT + (k * NB + tid) * 16 + 12) = base + pre[2 * k + 1];
        }
    }
    unsigned lens = __builtin_amdgcn_readfirstlane(*(const unsigned*)(smem + K2_LEN + w * 4));
    unsigned ordb = *(const unsigned*)(smem + K2_ORD + w * 16 + g * 4);
    const rsrc_t t2r = mk_rsrc(T2 + ((size_t)s * N + node0) * FS, (unsigned)(N - node0) * 256u);
    __syncthreads();
    STAMP(1, 8);

    u32x2 cs[SLOTS];
#pragma unroll
    for (int i = 0; i < 4; ++i) cs[i] = *(const LDSP u32x2*)(size_t)ca[i];
    if (flag[0]) {
        WAIT_V(0);
        k2_slow(smem, T1, T2, gw, ea, src, dst, out, N, E, s, node0);
        return;
    }
    {
        const unsigned lowm = (1u << (8 * (w & 3))) - 1u;
        asm volatile("s_waitcnt vmcnt(%3)\n\ts_cmp_ge_u32 %4, 4\n\ts_cbranch_scc1 .Lew_%=\n\ts_waitcnt vmcnt(0)\n.Lew_%=:"
                     : "+v"(re[0]), "+v"(re[1]), "+v"(cfv) : "i"(2 * EXP_PF), "s"(w) : "scc", "memory");
#define K2_SC(I) do {                                                                                                     \
            const unsigned p = __builtin_amdgcn_sad_u8(cs[I][0] & lowm, 0u, rk[I]) + cs[I][1];                            \
            const unsigned ov = (rm[(I) >> 2][(I) & 3] >> 16) << 8;                                                       \
            const u32x2 e2 = {re[(I) >> 1][((I) & 1) * 2], re[(I) >> 1][((I) & 1) * 2 + 1]};                              \
            asm volatile("s_mov_b64 exec, %4\n\tds_write_b32 %0, %1 offset:%c5\n\tds_write_b64 %2, %3 offset:%c6\n\ts_mov_b64 exec, -1" \
                         :: "v"(p * 4), "v"(ov), "v"(p * 8), "v"(e2), "s"(vm[I]), "i"(K2_OFFS), "i"(K2_EV) : "memory"); } while (0)
        K2_SC(0); K2_SC(1); K2_SC(2); K2_SC(3);
        if (big) {
#pragma unroll
            for (int i = 4; i < SLOTS; ++i) cs[i] = *(const LDSP u32x2*)(size_t)K2_CA(i);
            K2_SC(4); K2_SC(5); K2_SC(6); K2_SC(7);
        }
    }
    const rsrc_t outr = mk_rsrc(out + (size_t)node0 * D + s * FS, (unsigned)min(N - node0, NBN) * 512u - s * 128);
    WAIT_L0();
    __syncthreads();
    STAMP(1, 3);
    unsigned tot = (lens & 255) + ((lens >> 8) & 255) + ((lens >> 16) & 255);
    lens >>= 1;

    BLK_DECL;
#define V2(a, b) "v[" #a ":" #b "]"
#define K2_EXP2(a, b) "v_exp_f32 v" #a ", v" #a "\n\tv_exp_f32 v" #b ", v" #b "\n\t"
#define K2_RCP4(a, b, c, d) "v_rcp_f32 v" #a ", v" #a "\n\tv_rcp_f32 v" #b ", v" #b "\n\tv_rcp_f32 v" #c ", v" #c "\n\tv_rcp_f32 v" #d ", v" #d "\n\t"
#define K2_ADD1(x) "v_pk_add_f32 " x ", " x ", 1.0 op_sel_hi:[1,0]\n\t"
#define K2_FMA(d, x, y, z) "v_pk_fma_f32 " d ", " x ", " y ", " z "\n\t"
#define K2_RF(TL, TH, HL, HH)                                                     \
        K2_FMA("v[22:23]", "v[22:23]", TL, "v[22:23]") K2_FMA("v[24:25]", "v[24:25]", TH, "v[24:25]")         \
        K2_FMA("v[22:23]", HL, "v[104:105]", "v[22:23]") K2_FMA("v[24:25]", HH, "v[106:107]", "v[24:25]")     \
        K2_FMA("v[104:105]", "v[104:105]", TL, "v[104:105]") K2_FMA("v[106:107]", "v[106:107]", TH, "v[106:107]")
#define K2_FAST(A0, A1, A2, A3, B0, B1, B2, B3, HAL, HAH, HBL, HBH, ADD)          \
        K2_RF(V2(B0, B1), V2(B2, B3), HBL, HBH) ADD K2_RF(V2(A0, A1), V2(A2, A3), HAL, HAH)
#define K2_SAFE(A0, A1, A2, A3, B0, B1, B2, B3, HAL, HAH, HBL, HBH, ADD)          \
        K2_ADD1(V2(B0, B1)) K2_ADD1(V2(B2, B3)) K2_ADD1(V2(A0, A1)) K2_ADD1(V2(A2, A3)) \
        ADD                                                                       \
        K2_RCP4(A0, A1, A2, A3) K2_RCP4(B0, B1, B2, B3)                           \
        K2_FMA("v[22:23]", V2(A0, A1), HAL, "v[22:23]") K2_FMA("v[24:25]", V2(A2, A3), HAH, "v[24:25]") \
        K2_FMA("v[22:23]", V2(B0, B1), HBL, "v[22:23]") K2_FMA("v[24:25]", V2(B2, B3), HBH, "v[24:25]")
#define K2_YES(x) x
#define K2_NO(x) ""
#define K2_PAIR(L, MATH, LD, VW, A0, A1, A2, A3, B0, B1, B2, B3, NA, NB_, H0, H1, H2, H3, H4, H5, H6, H7, DNA, DNB, EK, ERD, AL, AH, ADD, ARD) \
        "s_sub_u32 s66, s66, 1\n\t"                                               \
        "s_cbranch_scc1 .Lsw" #L "_%=\n"                                          \
        ".Lrt" #L "_%=:\n\t"                                                      \
        LD("s_waitcnt lgkmcnt(2)\n\t")                                            \
        "v_mfma_f32_16x16x32_bf16 " NA ", " EK ", v[14:17], v[18:21]\n\t"         \
        K2_EXP2(B0, B1)                                                           \
        "v_mfma_f32_16x16x32_bf16 " NB_ ", " EK ", v[12:15], v[18:21]\n\t"        \
        ERD                                                                       \
        K2_EXP2(B2, B3)                                                           \
        "s_waitcnt " VW "\n\t"                                                    \
        "v_mfma_f32_16x16x32_bf16 " NA ", v[8:11], " DNA ", " NA "\n\t"           \
        K2_EXP2(A0, A1)                                                           \
        "v_mfma_f32_16x16x32_bf16 " NB_ ", v[8:11], " DNB ", " NB_ "\n\t"         \
        K2_EXP2(A2, A3)                                                           \
        MATH(A0, A1, A2, A3, B0, B1, B2, B3, V2(H0, H1), V2(H2, H3), V2(H4, H5), V2(H6, H7), LD(ADD)) \
        LD("buffer_load_dwordx4 " V2(H0, H3) ", " AL ", %[t1r], 0 offen\n\t")     \
        LD("buffer_load_dwordx4 " V2(H4, H7) ", " AH ", %[t1r], 0 offen\n\t")     \
        LD(ARD)
#define K2_BODY(L, MATH, LD, W0, W1, W2, W3)                                      \
        K2_PAIR(L##0, MATH, LD, W0, 30, 31, 32, 33, 34, 35, 36, 37, "v[96:99]", "v[100:103]", 64, 65, 66, 67, 68, 69, 70, 71,   \
                "v[72:75]", "v[76:79]", "v[42:45]", K2_EB(48), "v46", "v47", K2_ADDA, K2_AA(48))                                \
        K2_PAIR(L##1, MATH, LD, W1, 96, 97, 98, 99, 100, 101, 102, 103, "v[30:33]", "v[34:37]", 72, 73, 74, 75, 76, 77, 78, 79, \
                "v[80:83]", "v[84:87]", "v[38:41]", LD(K2_EA(64)), "v48", "v49", K2_ADDB, K2_AB(56))                            \
        K2_PAIR(L##2, MATH, LD, W2, 30, 31, 32, 33, 34, 35, 36, 37, "v[96:99]", "v[100:103]", 80, 81, 82, 83, 84, 85, 86, 87,   \
                "v[88:91]", "v[92:95]", "v[42:45]", LD(K2_EB(80)), "v46", "v47", K2_ADDA, K2_AA(64))                            \
        K2_PAIR(L##3, MATH, LD, W3, 96, 97, 98, 99, 100, 101, 102, 103, "v[30:33]", "v[34:37]", 88, 89, 90, 91, 92, 93, 94, 95, \
                "v[64:67]", "v[68:71]", "v[38:41]", LD(K2_EA(96)), "v48", "v49", K2_ADDB, K2_AB(72))
#define K2_LOOP(L, MATH)                                                          \
        ".Lloop" #L "_%=:\n\t"                                                    \
        K2_BODY(L, MATH, K2_YES, "vmcnt(4)", "vmcnt(4)", "vmcnt(4)", "vmcnt(4)")  \
        "v_add_u32 v53, 64, v53\n\t"                                              \
        "v_add_u32 v52, 32, v52\n\t"                                              \
        BLK_STAMP                                                                 \
          \
        "s_sub_u32 s65, s65, 8\n\t"                                               \
        "s_cmp_gt_i32 s65, %c[p1]\n\t"                                            \
        "s_cbranch_scc1 .Lloop" #L "_%=\n\t"                                      \
        "s_setprio 1\n\t"                                                         \
        "s_cmp_gt_i32 s65, 8\n\t"                                                 \
        "s_cbranch_scc1 .Lloop" #L "_%=\n\t"                                      \
        "s_setprio 0\n\t"                                                         \
          \
        "s_waitcnt lgkmcnt(0)\n\t"                                                \
        K2_BODY(L##t, MATH, K2_NO, "vmcnt(4)", "vmcnt(2) lgkmcnt(0)", "vmcnt(0)", "vmcnt(0)") \
        BLK_STAMP                                                                 \
        "s_branch .Lflush_%=\n"                                                   \
        K2_SW(L##0) K2_SW(L##1) K2_SW(L##2) K2_SW(L##3) K2_SW(L##t0) K2_SW(L##t1) K2_SW(L##t2) K2_SW(L##t3)
#define K2_SW(K) ".Lsw" #K "_%=:\n\ts_swappc_b64 s[62:63], s[60:61]\n\ts_branch .Lrt" #K "_%=\n"
#define K2_ADDA "v_pk_add_f32 v[46:47], v[46:47], v[50:51]\n\t"
#define K2_ADDB "v_pk_add_f32 v[48:49], v[48:49], v[50:51]\n\t"
#define K2_EA(O) "ds_read_b128 v[38:41], v53 offset:" #O "\n\t"
#define K2_EB(O) "ds_read_b128 v[42:45], v53 offset:" #O "\n\t"
#define K2_AA(O) "ds_read_b64 v[46:47], v52 offset:" #O "\n\t"
#define K2_AB(O) "ds_read_b64 v[48:49], v52 offset:" #O "\n\t"
#define K2_NEXT_T2                                                                \
        "v_and_b32 v57, 0xff, v54\n\t"                                            \
        "v_lshrrev_b32 v54, 8, v54\n\t"                                           \
        "v_lshl_add_u32 v58, v57, 8, v50\n\t"                                     \
        "v_lshl_add_u32 v59, v57, 9, v7\n\t"                                      \
        "buffer_load_dwordx4 v[26:29], v58, %[t2r], 0 offen\n\t"
#define K2_CIN  "v_lshlrev_b32 v18, 16, v26\n\tv_lshlrev_b32 v19, 16, v27\n\tv_lshlrev_b32 v20, 16, v28\n\tv_lshlrev_b32 v21, 16, v29\n\t"
#define K2_ACC  "v_mov_b64 v[22:23], v[26:27]\n\tv_mov_b64 v[24:25], v[28:29]\n\tv_mov_b32 v56, v59\n\t"    \
                "v_mov_b64 v[104:105], s[72:73]\n\tv_mov_b64 v[106:107], s[72:73]\n\t"
#define K2_LD(D0, D3, A) "buffer_load_dwordx4 v[" #D0 ":" #D3 "], v" #A ", %[t1r], 0 offen\n\t"
    asm volatile(
        "s_mov_b64 s[74:75], %c[safe0]\n"
        ".Lstart_%=:\n\t"
        "s_waitcnt lgkmcnt(0)\n\t"
        "ds_read_b128 v[30:33], %[vop]\n\t"
        "ds_read_b128 v[34:37], %[vop] offset:16\n\t"
        "v_mov_b32 v54, %[ordb]\n\t"
        "v_mov_b32 v53, %[vep]\n\t"
        "v_mov_b32 v52, %[vop]\n\t"
        "s_mov_b32 s64, %[lens]\n\t"
        "s_mov_b32 s65, %[tot]\n\t"
        K2_NEXT_T2
        K2_EA(0) K2_EB(16) K2_AA(32) K2_AB(40)
        "s_getpc_b64 s[60:61]\n"
        ".Lpc_%=:\n\t"
        "s_add_u32 s60, s60, .Lhnd_%=-.Lpc_%=\n\t"
        "s_addc_u32 s61, s61, 0\n\t"
        "s_and_b32 s66, s64, 0xff\n\t"
        "s_sub_u32 s66, s66, 1\n\t"
        "s_mov_b32 s67, 0\n\t"
        "s_mov_b32 s68, 0xbfb8aa3b\n\t"
        "s_mov_b32 s72, 1.0\n\t"
        "s_mov_b32 s73, 1.0\n\t"
        "s_mov_b32 s69, 0x7b800000\n\t"
        "v_mov_b32 v12, 0\n\t"
        "v_mov_b32 v13, 0\n\t"
        "v_mov_b32 v16, 0\n\t"
        "v_mov_b32 v17, 0\n\t"
        "s_waitcnt lgkmcnt(4)\n\t"
        "v_pk_add_f32 v[30:31], v[30:31], v[50:51]\n\t"
        "v_pk_add_f32 v[32:33], v[32:33], v[50:51]\n\t"
        "v_pk_add_f32 v[34:35], v[34:35], v[50:51]\n\t"
        "v_pk_add_f32 v[36:37], v[36:37], v[50:51]\n\t"
        K2_LD(64, 67, 30) K2_LD(68, 71, 31) K2_LD(72, 75, 32) K2_LD(76, 79, 33)
        "s_setprio 2\n\t"
        "s_waitcnt vmcnt(4) lgkmcnt(3)\n\t"
        K2_LD(80, 83, 34) K2_LD(84, 87, 35) K2_LD(88, 91, 36) K2_LD(92, 95, 37)
        K2_CIN K2_ACC
        K2_NEXT_T2
        "v_mfma_f32_16x16x32_bf16 v[30:33], v[38:41], v[14:17], v[18:21]\n\t"
        "v_mfma_f32_16x16x32_bf16 v[34:37], v[38:41], v[12:15], v[18:21]\n\t"
        K2_EA(32)
        "s_waitcnt vmcnt(7)\n\t"
        "v_mfma_f32_16x16x32_bf16 v[30:33], v[8:11], v[64:67], v[30:33]\n\t"
        "v_mfma_f32_16x16x32_bf16 v[34:37], v[8:11], v[68:71], v[34:37]\n\t"
        BLK_INIT
        "s_cmp_lg_u64 s[74:75], 0\n\t"
        "s_nop 2\n\t"
        "s_cbranch_scc1 .Lloopsf_%=\n"
        K2_LOOP(fa, K2_FAST)
        ".Lhnd_%=:\n\t"
        "s_cmp_lg_u32 s67, 0\n\t"
        "s_cbranch_scc1 .Lflush_%=\n\t"
        K2_CIN
        "s_mov_b32 s66, 0\n\t"
        "s_mov_b32 s67, 1\n\t"
        "s_setpc_b64 s[62:63]\n"
        ".Lflush_%=:\n\t"
        "v_pk_fma_f32 v[60:61], v[22:23], 0, v[104:105] op_sel_hi:[1,0,1]\n\t"
        "v_pk_fma_f32 v[62:63], v[24:25], 0, v[106:107] op_sel_hi:[1,0,1]\n\t"
        K2_RCP4(104, 105, 106, 107)
        "v_pk_add_f32 v[60:61], v[60:61], v[62:63]\n\t"
        "v_pk_mul_f32 v[22:23], v[22:23], v[104:105]\n\t"
        "v_add_f32 v60, v60, v61\n\t"
        "v_pk_mul_f32 v[24:25], v[24:25], v[106:107]\n\t"
        "v_cmp_nge_f32 vcc, s69, v60\n\t"
        "s_andn2_b64 vcc, vcc, s[74:75]\n\t"
        "s_cbranch_scc1 .Lredo_%=\n\t"
        "v_pk_mul_f32 v[60:61], v[22:23], s[68:69] op_sel_hi:[1,0]\n\t"
        "v_pk_mul_f32 v[62:63], v[24:25], s[68:69] op_sel_hi:[1,0]\n\t"
        "v_exp_f32 v60, v60\n\t"
        "v_exp_f32 v61, v61\n\t"
        "v_exp_f32 v62, v62\n\t"
        "v_exp_f32 v63, v63\n\t"
        "v_pk_add_f32 v[60:61], v[60:61], 1.0 op_sel_hi:[1,0]\n\t"
        "v_pk_add_f32 v[62:63], v[62:63], 1.0 op_sel_hi:[1,0]\n\t"
        "v_rcp_f32 v60, v60\n\t"
        "v_rcp_f32 v61, v61\n\t"
        "v_rcp_f32 v62, v62\n\t"
        "v_rcp_f32 v63, v63\n\t"
        "v_pk_mul_f32 v[60:61], v[60:61], v[22:23]\n\t"
        "v_pk_mul_f32 v[62:63], v[62:63], v[24:25]\n\t"
        "v_cvt_pk_bf16_f32 v62, v60, v62\n\t"
        "v_cvt_pk_bf16_f32 v63, v61, v63\n\t"
        "s_mov_b32 vcc_lo, 0x55555555\n\t"
        "s_mov_b32 vcc_hi, 0x55555555\n\t"
        "v_cndmask_b32_dpp v60, v63, v62, vcc quad_perm:[1,0,3,2] row_mask:0xf bank_mask:0xf\n\t"
        "s_not_b64 vcc, vcc\n\t"
        "v_cndmask_b32_dpp v61, v62, v63, vcc quad_perm:[1,0,3,2] row_mask:0xf bank_mask:0xf\n\t"
        "s_lshr_b32 s64, s64, 8\n\t"
        "buffer_store_dwordx2 v[60:61], v56, %[outr], 0 offen\n\t"
        "s_and_b32 s66, s64, 0xff\n\t"
        "s_cbranch_scc0 .Ldone_%=\n\t"
        K2_ACC
        K2_NEXT_T2
        "s_sub_u32 s66, s66, 2\n\t"
        "s_mov_b32 s67, 0\n\t"
        "s_setpc_b64 s[62:63]\n"
        ".Legc_code_end:\n"
        K2_LOOP(sf, K2_SAFE)
        ".Lredo_%=:\n\t"
        "s_waitcnt vmcnt(0)\n\t"
        "s_mov_b64 s[74:75], -1\n\t"
        "s_branch .Lstart_%=\n"
        ".Ldone_%=:\n\t"
        "s_waitcnt vmcnt(0) lgkmcnt(0)\n\t"
        BLK_LAST
        : BLK_OUT
        : [p1] "i"(EXP_P1), [safe0] "i"(EXP_SAFE ? -1 : 0), [lens] "s"(lens), [tot] "s"(tot), [vop] "v"(vop), [vep] "v"(vep),
          [ordb] "v"(ordb), [t1r] "s"(t1r), [t2r] "s"(t2r), [outr] "s"(outr), "{v[8:11]}"(asel), "{v[14:15]}"(cfv), "{v[50:51]}"(lo16),
          "{v7}"(olane)
        : "v12", "v13", "v16", "v17", "v18", "v19", "v20", "v21", "v22", "v23", "v24", "v25", "v26", "v27", "v28", "v29",
          "v30", "v31", "v32", "v33", "v34", "v35", "v36", "v37", "v38", "v39", "v40", "v41", "v42", "v43", "v44", "v45",
          "v46", "v47", "v48", "v49", "v52", "v53", "v54", "v56", "v57", "v58", "v59", "v60", "v61", "v62", "v63",
          "v64", "v65", "v66", "v67", "v68", "v69", "v70", "v71", "v72", "v73", "v74", "v75", "v76", "v77", "v78", "v79",
          "v80", "v81", "v82", "v83", "v84", "v85", "v86", "v87", "v88", "v89", "v90", "v91", "v92", "v93", "v94", "v95",
          "v96", "v97", "v98", "v99", "v100", "v101", "v102", "v103", "v104", "v105", "v106", "v107",
          "s60", "s61", "s62", "s63", "s64", "s65", "s66", "s67", "s68", "s69", "s70", "s71", "s72", "s73", "s74", "s75",
          "vcc", "scc", "memory");
    BLK_FLUSH;
    STAMP(1, 5);
    STAMP_FLUSH(1, 5);
    WSTAMP(0, stv[3]);
    WSTAMP(1, stv[5]);
    WSTAMP(2, tsteps);
    WSTAMP(3, (unsigned long long)__builtin_amdgcn_s_getreg(63492) | ((unsigned long long)__builtin_amdgcn_s_getreg(63508) << 32));
}

}

extern "C" void kernel_launch(void* const* d_in, const int* in_sizes, int n_in,
                              void* d_out, int out_size, void* d_ws, size_t ws_size,
                              hipStream_t stream) {
    const uint16_t* x  = (const uint16_t*)d_in[0];
    const uint16_t* ea = (const uint16_t*)d_in[1];
    const int* src     = (const int*)d_in[2];
    const int* dst     = (const int*)d_in[3];
    const uint16_t* uw = (const uint16_t*)d_in[4];
    const uint16_t* vw = (const uint16_t*)d_in[5];
    const uint16_t* gw = (const uint16_t*)d_in[6];
    const int N = in_sizes[0] / D;
    const int E = in_sizes[2];
    const int nrb   = (N + BM - 1) / BM;
    const int nbins = (N + NBN - 1) / NBN;
    int nchunks = 40;
    const int slots = nrb + nchunks / 8;
    const int ec = ((E + nchunks - 1) / nchunks + 3) & ~3;
    const int force_slow = (nbins > BINMAX || ec > RMAX * 2048) ? 1 : 0;
    if (force_slow) nchunks = 0;

    char* ws = (char*)d_ws;
    const size_t tb = ((size_t)NS * N * FS * 4 + 255) & ~(size_t)255;
    unsigned* T1 = (unsigned*)ws;
    unsigned* T2 = (unsigned*)(ws + tb);
    unsigned* recs = (unsigned*)(ws + 2 * tb);
    unsigned* tab = (unsigned*)(ws + 2 * tb + (((size_t)E * 12 + 16 + 255) & ~(size_t)255));

    (void)hipFuncSetAttribute((const void*)egc_k1, hipFuncAttributeMaxDynamicSharedMemorySize, K1_LDS);
    egc_k1<<<slots * 8, 512, K1_LDS, stream>>>(x, ea, src, dst, uw, vw, gw, T1, T2, recs, tab,
                                                 N, E, nrb, nbins, ec, nchunks, (N + nrb - 1) / nrb, NBK * 65536 / N);
    egc_k2<<<((nbins + 1) / 2) * 8, 512, 0, stream>>>(T1, T2, recs, tab, gw, ea, src, dst, (uint16_t*)d_out,
                                                      N, E, nchunks, nbins, ec, force_slow);
}
```

```cpp
#include <hip/hip_runtime.h>
#include <hip/hip_bf16.h>
#include <stdint.h>

typedef float    f32x4 __attribute__((ext_vector_type(4)));
typedef float    f32x2 __attribute__((ext_vector_type(2)));
typedef short    s16x4 __attribute__((ext_vector_type(4)));
typedef __bf16   bf16x8 __attribute__((ext_vector_type(8)));
typedef __bf16   bf16x2 __attribute__((ext_vector_type(2)));
typedef unsigned u32x4 __attribute__((ext_vector_type(4)));
typedef unsigned u32x3 __attribute__((ext_vector_type(3)));
typedef unsigned u32x2 __attribute__((ext_vector_type(2)));
typedef __amdgpu_buffer_rsrc_t rsrc_t;
#define LDSP __attribute__((address_space(3)))

#define BLK_DECL
#define BLK_INIT
#define BLK_STAMP
#define BLK_LAST
#define BLK_OUT
#define BLK_FLUSH
#define BLK_CLOB
#define STAMP_DECL
#define STAMP_ARG
#define STAMP_PASS
#define STAMP_FLUSH(K, L) do {} while (0)
#define STAMP(K, I) do {} while (0)
#define WSTAMP(I, V) do {} while (0)

#define AB_M(x) x
#define AB_G(x) x
#define AB_E(x) x
#define AB_T(x) x
#define NBK 4
#define EXP_PF 1
#define EXP_K1A 0
#define EXP_BSL 24
#define EXP_WROT 1
#define EXP_NT 0
#define EXP_STG 1
#define EXP_RPF 0
#define EXP_SAFE 0
#define EXP_OUTF " sc1"
#define EXP_K1B 1
#define EXP_OB "s_barrier\n\t"
#define EXP_R1E 0
#define EXP_XPF 0
#define EXP_T2T 1
#define EXP_T2D 7
#define EXP_PF0 1
#define EXP_NTW ""
#define EXP_NTX ""
#define EXP_DS0 0
#define EXP_P1 20
#define EXP_P2 10
#define EXP_ROT 1
namespace {

constexpr int D     = 256;
constexpr int FS    = 64;
constexpr int NS    = 4;
constexpr int BM    = 384;
constexpr int NB    = 80;
constexpr int NBN   = 79;
constexpr int CMAX  = 42;
constexpr int TSTR  = 64;
constexpr int RMAX  = 2;
constexpr int BINMAX = 256;
constexpr int SLOTS = 8;
constexpr int TPC   = 12;
constexpr int LSTR  = 72;
constexpr int MAXT  = 58;
constexpr unsigned MINL = 8;
constexpr float NLOG2E = -1.4426950408889634f;
constexpr unsigned SENT = 0x00fff000u;
constexpr unsigned OOB  = 0x7ffffff0u;
constexpr int AUX_WT = 16;

__device__ __forceinline__ rsrc_t mk_rsrc(const void* p, unsigned bytes) {
    return __builtin_amdgcn_make_buffer_rsrc((void*)p, (short)0, (int)bytes, 0x00020000);
}
__device__ __forceinline__ void dma16(rsrc_t r, unsigned voff, unsigned soff, unsigned lds) {
    asm volatile("s_mov_b32 m0, %0\n\ts_nop 0\n\tbuffer_load_dwordx4 %1, %2, %3 offen lds"
                 :: "s"(lds), "v"(voff), "s"(r), "s"(soff) : "m0", "memory");
}
__device__ __forceinline__ void dma16nt(rsrc_t r, unsigned voff, unsigned soff, unsigned lds) {
    asm volatile("s_mov_b32 m0, %0\n\ts_nop 0\n\tbuffer_load_dwordx4 %1, %2, %3 offen nt lds"
                 :: "s"(lds), "v"(voff), "s"(r), "s"(soff) : "m0", "memory");
}
#define WAIT_V(n) asm volatile("s_waitcnt vmcnt(%0)" :: "i"(n) : "memory")
#define WAIT_L0() asm volatile("s_waitcnt lgkmcnt(0)" ::: "memory")
#define BAR() __builtin_amdgcn_s_barrier()

__device__ __forceinline__ unsigned wave_scan(unsigned v) {
    v += __builtin_amdgcn_update_dpp(0u, v, 0x111, 0xf, 0xf, false);
    v += __builtin_amdgcn_update_dpp(0u, v, 0x112, 0xf, 0xf, false);
    v += __builtin_amdgcn_update_dpp(0u, v, 0x114, 0xf, 0xf, false);
    v += __builtin_amdgcn_update_dpp(0u, v, 0x118, 0xf, 0xf, false);
    v += __builtin_amdgcn_update_dpp(0u, v, 0x142, 0xa, 0xf, false);
    v += __builtin_amdgcn_update_dpp(0u, v, 0x143, 0xc, 0xf, false);
    return v;
}
__device__ __forceinline__ unsigned pack_bf16(float lo, float hi) {
    f32x2 p = {lo, hi};
    return __builtin_bit_cast(unsigned, __builtin_convertvector(p, bf16x2));
}
__device__ __forceinline__ float bf_lo(unsigned v) { return __builtin_bit_cast(float, v << 16); }
__device__ __forceinline__ float bf_hi(unsigned v) { return __builtin_bit_cast(float, v & 0xffff0000u); }

__device__ __forceinline__ unsigned pack_t1(float xb, float hv) {
    asm("" : "+v"(xb));
    const float p = xb * NLOG2E;
    unsigned P;
    asm("v_cvt_pk_bf16_f32 %0, %1, -%1" : "=v"(P) : "v"(p));
    const unsigned t = __builtin_bit_cast(unsigned, hv) - (unsigned)((int)P >> 16);
    return (t & 0xffff0000u) | (P & 0xffffu);
}

constexpr int K1_W   = 98304;
constexpr int K1_LDS = 163840;
__host__ __device__ constexpr int K1_R(int r) { return r == 5 ? 0 : r * 32768; }

__device__ __forceinline__ void k1_gemm(char* smem, const unsigned lds0, const int lane, const int w, const unsigned g0,
                                        const unsigned xvo, const rsrc_t xr, const rsrc_t tr STAMP_ARG) {
    const int g = lane >> 4, n = lane & 15;
    const int fh = w & 1, rg = w >> 1;
    const unsigned ldw = lds0 + w * 1024;
    unsigned gr[6];
#pragma unroll
    for (int r = 0; r < 6; ++r) gr[r] = (g0 >> (4 * r)) & 15u;
#define K1_LDX(r, i) (((EXP_NT & 4) || ((EXP_NT & 8) && (r) == 1)) ? dma16nt : dma16)(xr, xvo, gr[r] * 32768 + (i) * 8192, ldw + K1_R(r) + (i) * 8192)

    const f32x4 zero4 = {0.f, 0.f, 0.f, 0.f}, tiny4 = {1e-36f, 1e-36f, 1e-36f, 1e-36f};
    bf16x8 wf[8][4];
    bf16x8 xq[8];
    f32x4 acc[2][4];
    const unsigned fr = ((n >> 3) & 1) | ((g & 1) << 1);
    const char* wp0 = smem + K1_W + (8 * g + (n >> 2)) * 128 + (((2 * fh) ^ fr) << 5) + 8 * (n & 3);
    const char* wp1 = smem + K1_W + (8 * g + (n >> 2)) * 128 + (((2 * fh + 1) ^ fr) << 5) + 8 * (n & 3);
    unsigned xa[3][4];
#pragma unroll
    for (int k = 0; k < 4; ++k) {
        xa[0][k] = lds0 + n * 512 + rg * 8192 + (((4 * k + g) ^ n) << 4);
        xa[1][k] = xa[0][k] + 65536; xa[2][k] = xa[0][k] + 131072;
    }
    const unsigned ovo = g * 1024 + fh * 128 + n * 8 + rg * 4096;
#define K1_RDW(ks) do { _Pragma("unroll") for (int mf = 0; mf < 4; ++mf) {                                   \
        const char* wp = ((mf & 1) ? wp1 : wp0) + (ks) * 8192 + (mf >> 1) * 4096;                           \
        u32x2 l2 = __builtin_bit_cast(u32x2, __builtin_amdgcn_ds_read_tr16_b64_v4i16((LDSP s16x4*)wp));      \
        u32x2 h2 = __builtin_bit_cast(u32x2, __builtin_amdgcn_ds_read_tr16_b64_v4i16((LDSP s16x4*)(wp + 512))); \
        wf[ks][mf] = __builtin_bit_cast(bf16x8, u32x4{l2.x, l2.y, h2.x, h2.y}); } } while (0)
#define K1_RDX(r, ks) xq[ks] = *(const LDSP bf16x8*)(size_t)(xa[K1_R(r) >> 16][(ks) & 3] + (K1_R(r) & 65535) + ((ks) >> 2) * 256)
#define K1_MM(p, ks) do { _Pragma("unroll") for (int m = 0; m < 4; ++m)                                      \
        acc[p][m] = __builtin_amdgcn_mfma_f32_16x16x32_bf16(xq[ks], wf[ks][m], (ks) ? acc[p][m] : (m < 2 ? zero4 : tiny4), 0, 0, 0); } while (0)
#define SB() __builtin_amdgcn_sched_barrier(0)

    WAIT_V(4); BAR();
    STAMP(0, 2);
#pragma unroll
    for (int ks = 0; ks < 6; ++ks) K1_RDW(ks);
#pragma unroll
    for (int i = 0; i < 4; ++i) K1_LDX(1, i);
    SB();
    WAIT_V(4); BAR();
    STAMP(0, 15);
#pragma unroll
    for (int ks = 0; ks < 8; ++ks) K1_RDX(0, ks);
    K1_RDW(6); K1_RDW(7);
    SB();
#pragma unroll
    for (int ks = 0; ks < 8; ++ks) {
        if (ks == 5) {
            WAIT_L0(); WAIT_V(4); BAR();
            STAMP(0, 3);
#pragma unroll
            for (int k = 0; k < 5; ++k) K1_RDX(1, k);
        }
        K1_MM(0, ks);
        if (ks >= 5) K1_RDX(1, ks);
        if (ks < 4) K1_LDX(2, ks);
        SB();
    }
#pragma unroll
    for (int r = 1; r < 6; ++r) {
        const int p = r & 1, q = p ^ 1;
        if (r == 1) STAMP(0, 4);
        if (r == 2) STAMP(0, 8);
        if (r == 3) STAMP(0, 9);
        if (r == 4) STAMP(0, 11);
        if (r == 5) STAMP(0, 12);
        const unsigned vo = ovo + gr[r - 1] * 16384;
        u32x2 o, oh[2];
#pragma unroll
        for (int ks = 0; ks < 8; ++ks) {
            if (r < 5 && ks == 4) {
                WAIT_V(r == 1 ? (EXP_DS0 ? 4 : 6) : (r == 4 ? 4 : (r == 2 && EXP_DS0 ? 10 : 8))); BAR();
                if (EXP_DS0 && r == 1) {
                    __builtin_amdgcn_raw_buffer_store_b64(oh[0], tr, vo, 0, AUX_WT);
                    __builtin_amdgcn_raw_buffer_store_b64(oh[1], tr, vo + 256, 0, AUX_WT);
                }
#pragma unroll
                for (int k = 0; k < 4; ++k) K1_RDX(r + 1, k);
            }
            K1_MM(p, ks);
            if (r < 5 && ks >= 4) K1_RDX(r + 1, ks);
            o[ks & 1] = pack_t1(acc[q][ks & 1][ks >> 1], acc[q][2 + (ks & 1)][ks >> 1]);
            if (ks & 1) {
                if (EXP_DS0 && r == 1 && ks < 4) oh[ks >> 1] = o;
                else __builtin_amdgcn_raw_buffer_store_b64(o, tr, vo + (ks >> 1) * 256, 0, AUX_WT);
            }
            if (r <= 3 && ks < 4) K1_LDX(r + 2, ks);
#pragma unroll
            for (int i = 0; i < 4; ++i) {
                __builtin_amdgcn_sched_group_barrier(0x008, 1, 0);
                __builtin_amdgcn_sched_group_barrier(0x002, 1, 0);
            }
            SB();
        }
    }
    STAMP(0, 13);
#pragma unroll
    for (int r = 0; r < 4; ++r) {
        const u32x2 o = {pack_t1(acc[1][0][r], acc[1][2][r]), pack_t1(acc[1][1][r], acc[1][3][r])};
        __builtin_amdgcn_raw_buffer_store_b64(o, tr, ovo + gr[5] * 16384 + r * 256, 0, AUX_WT);
    }
#undef K1_LDX
}

__global__ __launch_bounds__(512, 2)
void egc_k1(const uint16_t* __restrict__ x, const uint16_t* __restrict__ ea,
            const int* __restrict__ src, const int* __restrict__ dst,
            const uint16_t* __restrict__ uw, const uint16_t* __restrict__ vw,
            const uint16_t* __restrict__ gw,
            unsigned* __restrict__ T1, unsigned* __restrict__ T2,
            unsigned* __restrict__ recs, unsigned* __restrict__ tab,
            int N, int E, int nrb, int nbins, int ec, int nchunks, int rows, int bmul)
{
    extern __shared__ __attribute__((aligned(16))) char smem[];
    const int b   = blockIdx.x;
    const int slot = b >> 3;
    const int item = (b & 7) * nrb + slot;
    const int rb  = item >> 3;
    const int ct  = item & 7;
    const int tid = threadIdx.x;
    const int lane = tid & 63;
    const int w = __builtin_amdgcn_readfirstlane(tid >> 6);
    asm volatile(".Legc_code_begin:" :: "s"(x), "s"(ea), "s"(src), "s"(dst), "s"(uw), "s"(vw), "s"(gw), "s"(T1), "s"(T2), "s"(recs), "s"(tab),
                       "s"(N), "s"(E), "s"(nrb), "s"(nbins), "s"(ec), "s"(nchunks), "s"(rows), "s"(bmul));
    STAMP_DECL;
    STAMP(0, 0);

    const int s = ct >> 1;
    const int row0 = rb * rows;
    const unsigned nr = (unsigned)max(min(rows, N - row0), 0);
    const unsigned lds0 = (unsigned)(size_t)(LDSP char*)smem;
    const rsrc_t xr = mk_rsrc(x + (size_t)row0 * D, nr * 512u);
    const unsigned rl = 2 * w + (lane >> 5);
    const unsigned xvo = rl * 512 + (((lane & 31) ^ rl) << 4);
    const unsigned g0 = (unsigned)(0x2154321543210ull >> (4 * ct)) & ~15u;
    {
        const uint16_t* wsel = (ct & 1) ? (w < 4 ? gw : uw) : (w < 4 ? gw + D * D : vw);
        const unsigned f = ((lane >> 4) & 1) | ((w & 1) << 1);
        const unsigned wvo = (lane >> 3) * 512 + s * 128 + ((((lane >> 1) & 3) ^ f) << 5) + (lane & 1) * 16 + (w & 3) * 4096;
        unsigned t0, t1;
#define K1_W1 "s_lshl_b32 %1, %0, 1\n\ts_add_u32 m0, %0, %[lw]\n\ts_add_u32 %0, %0, 0x2000\n\t"                  \
              "buffer_load_dwordx4 %[wvo], %[wr], %1 offen" EXP_NTW " lds\n\ts_and_b32 %0, %0, 0xe000\n\t"
#define K1_X1(O) "s_add_u32 m0, %[lx], " #O "\n\ts_add_u32 %1, %[gx], " #O "\n\tbuffer_load_dwordx4 %[xvo], %[xr], %1 offen" EXP_NTX " lds\n\t"
        asm volatile("s_cmp_ge_i32 %[slot], %[nrb]\n\t"
                     "s_cbranch_scc1 .Lk1skip_%=\n\t"
                     "s_mov_b32 %0, %[k0]\n\t"
                     K1_W1 K1_W1 K1_W1 K1_W1 K1_W1 K1_W1 K1_W1 K1_W1
                     EXP_OB
                     K1_X1(0) K1_X1(0x2000) K1_X1(0x4000) K1_X1(0x6000)
                     ".Lk1skip_%=:"
                     : "=&s"(t0), "=&s"(t1)
                     : [slot] "s"(slot), [nrb] "s"(nrb), [k0] "s"(EXP_WROT * (rb & 3) * 16384), [lw] "s"(lds0 + w * 1024 + K1_W),
                       [wvo] "v"(wvo), [wr] "s"(mk_rsrc(wsel, D * D * 2)), [lx] "s"(lds0 + w * 1024), [gx] "s"((g0 & 15u) * 32768),
                       [xvo] "v"(xvo), [xr] "s"(xr)
                     : "m0", "scc", "memory");
    }
    STAMP(0, 1);
    if (slot < nrb) {
        k1_gemm(smem, lds0, lane, w, g0, xvo, xr,
                mk_rsrc(((ct & 1) ? T2 : T1) + ((size_t)s * N + row0) * FS, nr * 256u) STAMP_PASS);
        STAMP(0, 5);
        STAMP(0, 6);
        STAMP_FLUSH(0, 6);
        return;
    }
    {
        const int c = (slot - nrb) * 8 + (b & 7);
        unsigned cpf;
        {
            unsigned long long pc;
            unsigned len;
            unsigned back;
            asm volatile("s_getpc_b64 %0\n.Legc_code_here:\n\ts_mov_b32 %1, .Legc_code_end-.Legc_code_begin\n\t"
                         "s_mov_b32 %2, .Legc_code_here-.Legc_code_begin" : "=s"(pc), "=s"(len), "=s"(back));
            pc -= back;
            if (back > 65536u) len = 0;
            cpf = __builtin_amdgcn_raw_buffer_load_b32(mk_rsrc((const void*)pc, len > 65536u ? 0u : len), tid * 64, 0, 0);
        }
        if (c == 0 && tid < 256) {
            const int g = lane >> 4, n = lane & 15;
            const uint16_t* cw = gw + 2 * D * D + w * FS + (n >> 3) * 32 + (n & 7) * 2 + (g >> 1) + (g & 1) * 16;
            const u32x2 cf = {pack_bf16(bf_lo(cw[0]) * NLOG2E, bf_lo(cw[D]) * NLOG2E),
                              pack_bf16(bf_lo(cw[2 * D]) * NLOG2E, bf_lo(cw[3 * D]) * NLOG2E)};
            __builtin_amdgcn_raw_buffer_store_b64(cf, mk_rsrc(tab + BINMAX * TSTR, 2048), tid * 8, 0, AUX_WT);
        }
        if (c >= nchunks) { asm volatile("" :: "v"(cpf)); return; }
        unsigned* cw  = (unsigned*)smem;
        unsigned* wt  = cw + 8 * BINMAX;
        unsigned* rme = wt + 16;
        u32x2*    rev = (u32x2*)(rme + RMAX * 2048);
        const int e0 = c * ec;
        const int ne = max(min(E - e0, ec), 0);
        const rsrc_t dr = mk_rsrc(dst + e0, ne * 4), sr = mk_rsrc(src + e0, ne * 4), er = mk_rsrc(ea + (size_t)e0 * 4, ne * 8);
        u32x4 dv[RMAX], sv[RMAX], ev[RMAX][2];
#pragma unroll
        for (int r = 0; r < RMAX; ++r) dv[r] = __builtin_amdgcn_raw_buffer_load_b128(dr, tid * 16, r * 8192, 2);
        ((u32x4*)cw)[tid] = u32x4{0u, 0u, 0u, 0u};
        __builtin_amdgcn_sched_barrier(0);
        __builtin_amdgcn_s_sleep(EXP_BSL);
        __builtin_amdgcn_sched_barrier(0);
#pragma unroll
        for (int r = 0; r < RMAX; ++r) {
            sv[r] = __builtin_amdgcn_raw_buffer_load_b128(sr, tid * 16, r * 8192, 2);
            ev[r][0] = __builtin_amdgcn_raw_buffer_load_b128(er, tid * 32, r * 16384, 2);
            ev[r][1] = __builtin_amdgcn_raw_buffer_load_b128(er, tid * 32 + 16, r * 16384, 2);
        }
        __builtin_amdgcn_sched_barrier(0);
        __syncthreads();
        asm volatile("" :: "v"(cpf));
        unsigned* cmine = cw + w * BINMAX;
        unsigned rk[RMAX][4], bn[RMAX][4];
#pragma unroll
        for (int r = 0; r < RMAX; ++r)
#pragma unroll
            for (int k = 0; k < 4; ++k) {
                bn[r][k] = __umul24(dv[r][k], 53093u) >> 22;
                rk[r][k] = 0;
                if (r * 2048 + tid * 4 + k < ne) rk[r][k] = atomicAdd(&cmine[bn[r][k]], 1u);
            }
        __syncthreads();
        STAMP(2, 1);
        {
            unsigned pre[8], t = 0, inc = 0;
            if (tid < BINMAX) {
#pragma unroll
                for (int k = 0; k < 8; ++k) { pre[k] = t; t += cw[k * BINMAX + tid]; }
                inc = wave_scan(t);
                if (lane == 63) wt[w] = inc;
            }
            __syncthreads();
            if (tid < BINMAX) {
                const u32x4 w4 = *(const u32x4*)wt;
                const unsigned st = inc - t + (w > 0 ? w4[0] : 0u) + (w > 1 ? w4[1] : 0u) + (w > 2 ? w4[2] : 0u);
#pragma unroll
                for (int k = 0; k < 8; ++k) cw[k * BINMAX + tid] = st + pre[k];
                if (tid < nbins) __builtin_amdgcn_raw_buffer_store_b32(st | (t << 16), mk_rsrc(tab, BINMAX * TSTR * 4), (tid * TSTR + c) * 4, 0, AUX_WT);
            }
        }
        __syncthreads();
        STAMP(2, 2);
#pragma unroll
        for (int r = 0; r < RMAX; ++r)
#pragma unroll
            for (int k = 0; k < 4; ++k) rk[r][k] += cmine[bn[r][k]];
#pragma unroll
        for (int r = 0; r < RMAX; ++r)
#pragma unroll
            for (int k = 0; k < 4; ++k)
                if (r * 2048 + tid * 4 + k < ne) {
                    rev[rk[r][k]] = u32x2{ev[r][k >> 1][(k & 1) * 2], ev[r][k >> 1][(k & 1) * 2 + 1]};
                    rme[rk[r][k]] = (sv[r][k] << 16) + (dv[r][k] - bn[r][k] * NBN + (__umul24(sv[r][k], bmul) >> 16) * NB) * 16u;
                }
        __syncthreads();
        STAMP(2, 3);
        const rsrc_t omr = mk_rsrc(recs + e0, ne * 4), oer = mk_rsrc(recs + (size_t)((E + 3) & ~3) + (size_t)e0 * 2, ne * 8);
#pragma unroll
        for (int i = 0; i < RMAX * 3; ++i) {
            const u32x4 v = ((const u32x4*)rme)[tid + 512 * i];
            __builtin_amdgcn_raw_buffer_store_b128(v, i < RMAX ? omr : oer, tid * 16 + (i < RMAX ? i : i - RMAX) * 8192, 0, AUX_WT);
        }
        STAMP(2, 5);
        STAMP(2, 6);
        STAMP_FLUSH(2, 6);
    }
}

constexpr int K2_OFFS = 0;
constexpr int K2_EV   = K2_OFFS + 32 * LSTR * 4;
constexpr int K2_CNT  = K2_EV + 33 * LSTR * 8;
constexpr int K2_HIST = K2_CNT + NBK * NB * 16;
constexpr int K2_ORD  = K2_HIST + 256;
constexpr int K2_LEN  = K2_ORD + 128;
constexpr int K2_FLAG = K2_LEN + 32;
constexpr int K2_PF   = K2_FLAG + 16;
constexpr int K2_LDS  = K2_PF + 8192;
static_assert((K2_PF - K2_EV) % 16 == 0 && K2_EV % 16 == 0, "LDS map");

__device__ __forceinline__ u32x2 silu_pack(f32x4 y) {
    f32x4 o;
#pragma unroll
    for (int r = 0; r < 4; ++r) {
        float t = __builtin_amdgcn_exp2f(y[r] * NLOG2E);
        o[r] = y[r] * __builtin_amdgcn_rcpf(1.0f + t);
    }
    return u32x2{pack_bf16(o[0], o[1]), pack_bf16(o[2], o[3])};
}

__device__ __forceinline__ void k2_slow(char* smem, const unsigned* __restrict__ T1, const unsigned* __restrict__ T2,
                                        const uint16_t* __restrict__ gw, const uint16_t* __restrict__ ea,
                                        const int* __restrict__ src, const int* __restrict__ dst,
                                        uint16_t* __restrict__ out, int N, int E, int s, int node0) {
    float* accs = (float*)smem;
    unsigned* queue = (unsigned*)(smem + NB * FS * 4);
    static_assert(NB * FS * 4 + 2048 <= K2_CNT, "slow-path LDS");
    const unsigned* t2g = T2 + ((size_t)s * N + node0) * FS;
    const int tid = threadIdx.x, f = tid & 63, w = tid >> 6;
    __syncthreads();
    for (int i = tid; i < NB * FS; i += 512) accs[i] = 0.f;
    const uint16_t* cf = gw + (size_t)2 * D * D + s * FS + (f & 32) + (f & 1) * 16 + ((f & 31) >> 1);
    const float c0 = bf_lo(cf[0]) * NLOG2E, c1 = bf_lo(cf[D]) * NLOG2E;
    const float c2 = bf_lo(cf[2 * D]) * NLOG2E, c3 = bf_lo(cf[3 * D]) * NLOG2E;
#pragma unroll 1
    for (int eb = 0; eb < E; eb += 512) {
        __syncthreads();
        const int e = eb + tid;
        const int dn = (e < E) ? dst[e] - node0 : -1;
        queue[tid] = (dn >= 0 && dn < NBN) ? (unsigned)dn : 0xffffffffu;
        __syncthreads();
#pragma unroll 1
        for (int q0 = 0; q0 < 512; q0 += 64) {
            const unsigned qv = queue[q0 + f];
            unsigned long long m = __ballot(qv != 0xffffffffu && (qv & 7u) == (unsigned)w);
            while (m) {
                const int l = __builtin_ctzll(m);
                m &= m - 1;
                const int ee = eb + q0 + l;
                const int d2 = __builtin_amdgcn_readlane(qv, l);
                const unsigned tv = T1[((size_t)s * N + src[ee]) * FS + f];
                const u32x2 e2 = *(const u32x2*)(ea + (size_t)ee * 4);
                const float z = bf_lo(t2g[d2 * FS + f]) + bf_lo(tv) + bf_lo(e2.x) * c0 + bf_hi(e2.x) * c1
                                + bf_lo(e2.y) * c2 + bf_hi(e2.y) * c3;
                accs[d2 * FS + f] += __builtin_bit_cast(float, tv) * __builtin_amdgcn_rcpf(1.0f + __builtin_amdgcn_exp2f(z));
            }
        }
    }
    __syncthreads();
#pragma unroll 1
    for (int p = tid; p < NBN * 16; p += 512) {
        const int r = p >> 4, q4 = (p & 15) * 4;
        if (node0 + r < N) {
            f32x4 y;
#pragma unroll
            for (int k = 0; k < 4; ++k) {
                const int sl = r * FS + (q4 & 32) + ((q4 & 15) + k) * 2 + ((q4 >> 4) & 1);
                y[k] = accs[sl] + __builtin_bit_cast(float, t2g[sl]);
            }
            *(u32x2*)(out + (size_t)(node0 + r) * D + s * FS + q4) = silu_pack(y);
        }
    }
}

__global__ __launch_bounds__(512, 4)
void egc_k2(const unsigned* __restrict__ T1, const unsigned* __restrict__ T2,
            const unsigned* __restrict__ recs, const unsigned* __restrict__ tab,
            const uint16_t* __restrict__ gw, const uint16_t* __restrict__ ea,
            const int* __restrict__ src, const int* __restrict__ dst,
            uint16_t* __restrict__ out, int N, int E, int nchunks, int nbins, int ec, int force_slow)
{
    __shared__ __attribute__((aligned(16))) char smem[K2_LDS];
    unsigned* hist = (unsigned*)(smem + K2_HIST);
    unsigned* flag = (unsigned*)(smem + K2_FLAG);

    asm volatile("" :: "s"(T1), "s"(T2), "s"(recs), "s"(tab), "s"(out), "s"(N), "s"(E), "s"(nchunks),
                       "s"(nbins), "s"(ec), "s"(force_slow));
    const int b   = blockIdx.x;
    const int s   = (b & 7) >> 1;
    const int hb  = (nbins + 1) >> 1;
    const int bin = (b & 1) * hb + (b >> 3);
    const int tid = threadIdx.x;
    const int lane = tid & 63;
    const int w = __builtin_amdgcn_readfirstlane(tid >> 6);
    const int g = lane >> 4, n = lane & 15;
    const int node0 = bin * NBN;
    const unsigned lds0 = (unsigned)(size_t)(LDSP char*)smem;
    const rsrc_t t1r = mk_rsrc(T1 + (size_t)s * N * FS, (unsigned)N * 256u);
    __builtin_amdgcn_s_setprio(3);
    STAMP_DECL;
    STAMP(1, 0);

    const unsigned c = __umul24(tid, 5462u) >> 16, j = tid - c * TPC;
    unsigned te;
    asm volatile("buffer_load_dword %0, %1, %2, 0 offen"
                 : "=&v"(te) : "v"(c * 4), "s"(mk_rsrc(tab + bin * TSTR, bin < nbins ? nchunks * 4 : 0)) : "memory");
#pragma unroll
    for (int i = 0; i < EXP_PF0; ++i)
        dma16(t1r, (b >> 3) * 8192u + tid * 16u, hb * 8192u * i, lds0 + K2_PF + w * 1024);
    {
        const u32x4 sv = {SENT, SENT, SENT, SENT}, zv = {0u, 0u, 0u, 0u};
        u32x4* po = (u32x4*)(smem + K2_OFFS);
        constexpr int NO = 32 * LSTR / 4, NZ = (K2_PF - K2_EV) / 16;
        po[tid] = sv;
        if (tid < NO - 512) po[tid + 512] = sv;
        u32x4* pz = (u32x4*)(smem + K2_EV);
#pragma unroll
        for (int i = 0; i < NZ / 512; ++i) pz[tid + 512 * i] = zv;
        if (tid < NZ % 512) pz[tid + NZ / 512 * 512] = zv;
    }
    u32x4 asel;
#pragma unroll
    for (int k = 0; k < 4; ++k) asel[k] = (n == 4 * g + k) ? 0x3f80u : 0u;
    u32x2 lo16 = {(unsigned)n * 16u, (unsigned)n * 16u};
    unsigned vop = lds0 + K2_OFFS + (w * 4 + g) * (LSTR * 4);
    unsigned vep = lds0 + K2_EV + (((n & 3) == g) ? w * 4 + (n >> 2) : 32) * (LSTR * 8);
    unsigned olane = (n >> 3) * 64 + (n & 6) * 4 + (n & 1) * 32;
    unsigned j4 = 4 * j, rb0 = __umul24(c, ec) + j4;
    const rsrc_t mr = mk_rsrc(recs, (unsigned)E * 4u), er = mk_rsrc(recs + ((E + 3) & ~3), (unsigned)E * 8u);
    asm volatile("s_waitcnt vmcnt(%10)" : "+v"(te), "+v"(asel), "+v"(lo16), "+v"(vop), "+v"(vep), "+v"(olane), "+v"(j4), "+v"(rb0)
                 : "s"(mr), "s"(er), "i"(EXP_PF0) : "memory");
    STAMP(1, 9);
    const int nc = (int)(te >> 16) - (int)j4;
    u32x4 rm[2], re[4];
    const unsigned r0 = nc > 0 ? (rb0 + (te & 0xffffu)) * 4u : OOB / 2;
    asm volatile("buffer_load_dwordx4 %0, %1, %2, 0 offen" : "=&v"(rm[0]) : "v"(r0), "s"(mr) : "memory");
    __builtin_amdgcn_sched_barrier(0);
    unsigned long long vm[SLOTS];
#pragma unroll
    for (int i = 0; i < SLOTS; ++i) vm[i] = __builtin_amdgcn_ballot_w64(nc > (i >> 2) * (4 * TPC) + (i & 3));
    const bool big = vm[4] != 0;
    asm volatile("s_cmp_eq_u64 %3, 0\n\t"
                 "s_cbranch_scc1 .Lnb_%=\n\t"
                 "buffer_load_dwordx4 %0, %1, %2, 0 offen offset:192\n"
                 ".Lnb_%=:"
                 : "=&v"(rm[1]) : "v"(r0), "s"(mr), "s"(vm[4]) : "scc", "memory");
    if (bin >= nbins) { WAIT_V(0); return; }
    __syncthreads();
    STAMP(1, 1);
    if (nc > TPC * SLOTS || force_slow) flag[0] = 1u;
    asm volatile("s_waitcnt vmcnt(0)" : "+v"(rm[0]), "+v"(rm[1]) :: "memory");
    STAMP(1, 10);
    u32x2 cfv;
    asm volatile("s_cmp_eq_u64 %4, 0\n\t"
                 "s_cbranch_scc1 .Lnb_%=\n\t"
                 "buffer_load_dwordx4 %0, %2, %3, 0 offen offset:384\n\t"
                 "buffer_load_dwordx4 %1, %2, %3, 0 offen offset:400\n"
                 ".Lnb_%=:"
                 : "=&v"(re[2]), "=&v"(re[3]) : "v"(r0 * 2), "s"(er), "s"(vm[4]) : "scc", "memory");
    asm volatile("buffer_load_dwordx4 %0, %3, %5, 0 offen\n\t"
                 "buffer_load_dwordx4 %1, %3, %5, 0 offen offset:16\n\t"
                 "buffer_load_dwordx2 %2, %4, %6, 0 offen"
                 : "=&{v[108:111]}"(re[0]), "=&{v[112:115]}"(re[1]), "=&{v[14:15]}"(cfv)
                 : "v"(r0 * 2), "v"(lane * 8), "s"(er), "s"(mk_rsrc(tab + BINMAX * TSTR + s * 128, 512)) : "memory");
    unsigned ca[SLOTS], rk[SLOTS];
#define K2_CA(I) (lds0 + K2_CNT + (w & 4) * 2 + (rm[(I) >> 2][(I) & 3] & 0xffffu))
#pragma unroll
    for (int i = 0; i < 4; ++i) ca[i] = K2_CA(i);
#define K2_AT(R, A, M) "s_mov_b64 exec, %[" #M "]\n\tds_add_rtn_u32 %[" #R "], %[" #A "], %[one]\n\t"
#define K2_AT4(O) asm volatile(K2_AT(r0, a0, m0) K2_AT(r1, a1, m1) K2_AT(r2, a2, m2) K2_AT(r3, a3, m3)                   \
                 "s_mov_b64 exec, -1\n\ts_waitcnt lgkmcnt(0)"                                                            \
                 : [r0] "=&v"(rk[O]), [r1] "=&v"(rk[O + 1]), [r2] "=&v"(rk[O + 2]), [r3] "=&v"(rk[O + 3])                \
                 : [a0] "v"(ca[O]), [a1] "v"(ca[O + 1]), [a2] "v"(ca[O + 2]), [a3] "v"(ca[O + 3]),                       \
                   [m0] "s"(vm[O]), [m1] "s"(vm[O + 1]), [m2] "s"(vm[O + 2]), [m3] "s"(vm[O + 3]),                       \
                   [one] "v"(1u << (8 * (w & 3))) : "memory")
    K2_AT4(0);
    asm volatile("" : "=v"(rk[4]), "=v"(rk[5]), "=v"(rk[6]), "=v"(rk[7]));
    if (big) {
#pragma unroll
        for (int i = 4; i < SLOTS; ++i) ca[i] = K2_CA(i);
        K2_AT4(4);
    }
    __syncthreads();
    STAMP(1, 2);
    if (w >= 4) {
#pragma unroll
        for (int i = 0; i < 2 * EXP_PF; ++i)
            dma16(t1r, (b >> 3) * 4096u + (tid - 256) * 16u, hb * 4096u * (i + 2 * EXP_PF0), lds0 + K2_PF + w * 1024);
    }

    unsigned pre[2 * NBK], deg = 0, key = 0, rank = 0;
    if (tid < NB) {
        u32x4 f4[NBK];
#pragma unroll
        for (int k = 0; k < NBK; ++k) f4[k] = *(const u32x4*)(smem + K2_CNT + (k * NB + tid) * 16);
#pragma unroll
        for (int k = 0; k < NBK; ++k) {
            pre[2 * k] = deg;
            pre[2 * k + 1] = __builtin_amdgcn_sad_u8(f4[k][0], 0u, deg);
            deg = __builtin_amdgcn_sad_u8(f4[k][2], 0u, pre[2 * k + 1]);
        }
        key = 63u - min(deg, 63u);
        rank = (atomicAdd(&hist[key], 1u << (16 * w)) >> (16 * w)) & 0xffffu;
    }
    {
        unsigned long long bad = 0;
#pragma unroll
        for (int i = 0; i < 4; ++i) {
            rk[i] = (rk[i] >> (8 * (w & 3))) & 255u;
            bad |= __builtin_amdgcn_ballot_w64(rk[i] > 63u) & vm[i];
        }
        if (big) {
#pragma unroll
            for (int i = 4; i < SLOTS; ++i) {
                rk[i] = (rk[i] >> (8 * (w & 3))) & 255u;
                bad |= __builtin_amdgcn_ballot_w64(rk[i] > 63u) & vm[i];
            }
        }
        if (bad) flag[0] = 1u;
    }
    __syncthreads();
    STAMP(1, 6);
    unsigned wv = 0, pos = 0;
    if (w < 2) {
        const unsigned hv = hist[lane], h1 = hv >> 16, ht = (hv & 0xffffu) + h1;
        const unsigned ex = wave_scan(ht) - (w ? h1 : ht);
        rank += __builtin_amdgcn_ds_bpermute(key * 4, ex);
        pos = rank >> 4;
        wv = ((rank >> 2) & 3u) ^ (pos ? 3u : 0u);
        if (pos >= 2) { wv += 4; pos -= 2; }
        if (tid < NB) {
            smem[K2_ORD + wv * 16 + (rank & 3u) * 4 + pos] = (char)tid;
            if ((rank & 3u) == 0) smem[K2_LEN + wv * 4 + pos] = (char)min(max((deg + 1u) & ~1u, MINL), 254u);
        }
    }
    __syncthreads();
    STAMP(1, 7);
    if (tid < NB) {
        const unsigned l4 = *(const unsigned*)(smem + K2_LEN + wv * 4);
        const unsigned first = __builtin_amdgcn_sad_u8(l4 & ((1u << (8 * pos)) - 1u), 0u, 0u);
        const unsigned base = (wv * 4 + (rank & 3u)) * LSTR + first;
        if (first + max(deg, MINL) > MAXT) flag[0] = 1u;
#pragma unroll
        for (int k = 0; k < NBK; ++k) {
            *(unsigned*)(smem + K2_CNT + (k * NB + tid) * 16 + 4) = base + pre[2 * k];
            *(unsigned*)(smem + K2_CNT + (k * NB + tid) * 16 + 12) = base + pre[2 * k + 1];
        }
    }
    unsigned lens = __builtin_amdgcn_readfirstlane(*(const unsigned*)(smem + K2_LEN + w * 4));
    unsigned ordb = *(const unsigned*)(smem + K2_ORD + w * 16 + g * 4);
    const rsrc_t t2r = mk_rsrc(T2 + ((size_t)s * N + node0) * FS, (unsigned)(N - node0) * 256u);
    if (EXP_T2T) dma16(t2r, (ordb & 255u) * 256u + lo16[0], 0, lds0 + K2_PF + w * 1024);
    __syncthreads();
    STAMP(1, 8);

    u32x2 cs[SLOTS];
#pragma unroll
    for (int i = 0; i < 4; ++i) cs[i] = *(const LDSP u32x2*)(size_t)ca[i];
    const unsigned slow = __builtin_amdgcn_readfirstlane(flag[0]);
#pragma unroll
    for (int i = 0; i < SLOTS; ++i) vm[i] = slow ? 0ull : vm[i];
    unsigned p8[4];
    {
        const unsigned lowm = (1u << (8 * (w & 3))) - 1u;
#pragma unroll
        for (int i = 0; i < 4; ++i) {
            const unsigned p = __builtin_amdgcn_sad_u8(cs[i][0] & lowm, 0u, rk[i]) + cs[i][1];
            asm volatile("s_mov_b64 exec, %2\n\tds_write_b32 %0, %1 offset:%c3\n\ts_mov_b64 exec, -1"
                         :: "v"(p * 4), "v"((rm[0][i] >> 16) << 8), "s"(vm[i]), "i"(K2_OFFS) : "memory");
            p8[i] = p * 8;
        }
        if (big) {
            asm volatile("s_waitcnt vmcnt(3)" : "+v"(re[2]), "+v"(re[3]) :: "memory");
#pragma unroll
            for (int i = 4; i < SLOTS; ++i) cs[i] = *(const LDSP u32x2*)(size_t)K2_CA(i);
#pragma unroll
            for (int i = 4; i < SLOTS; ++i) {
                const unsigned p = __builtin_amdgcn_sad_u8(cs[i][0] & lowm, 0u, rk[i]) + cs[i][1];
                const u32x2 e2 = {re[i >> 1][(i & 1) * 2], re[i >> 1][(i & 1) * 2 + 1]};
                asm volatile("s_mov_b64 exec, %4\n\tds_write_b32 %0, %1 offset:%c5\n\tds_write_b64 %2, %3 offset:%c6\n\ts_mov_b64 exec, -1"
                             :: "v"(p * 4), "v"((rm[1][i & 3] >> 16) << 8), "v"(p * 8), "v"(e2), "s"(vm[i]), "i"(K2_OFFS), "i"(K2_EV) : "memory");
            }
        }
    }
    const rsrc_t outr = mk_rsrc(out + (size_t)node0 * D + s * FS, (unsigned)min(N - node0, NBN) * 512u - s * 128);
    WAIT_L0();
    __syncthreads();
    STAMP(1, 3);
    unsigned tot = (lens & 255) + ((lens >> 8) & 255) + ((lens >> 16) & 255);
    lens >>= 1;

    BLK_DECL;
#define V2(a, b) "v[" #a ":" #b "]"
#define K2_EXP2(a, b) "v_exp_f32 v" #a ", v" #a "\n\tv_exp_f32 v" #b ", v" #b "\n\t"
#define K2_RCP4(a, b, c, d) "v_rcp_f32 v" #a ", v" #a "\n\tv_rcp_f32 v" #b ", v" #b "\n\tv_rcp_f32 v" #c ", v" #c "\n\tv_rcp_f32 v" #d ", v" #d "\n\t"
#define K2_ADD1(x) "v_pk_add_f32 " x ", " x ", 1.0 op_sel_hi:[1,0]\n\t"
#define K2_FMA(d, x, y, z) "v_pk_fma_f32 " d ", " x ", " y ", " z "\n\t"
#define K2_RF(TL, TH, HL, HH)                                                     \
        K2_FMA("v[22:23]", "v[22:23]", TL, "v[22:23]") K2_FMA("v[24:25]", "v[24:25]", TH, "v[24:25]")         \
        K2_FMA("v[22:23]", HL, "v[104:105]", "v[22:23]") K2_FMA("v[24:25]", HH, "v[106:107]", "v[24:25]")     \
        K2_FMA("v[104:105]", "v[104:105]", TL, "v[104:105]") K2_FMA("v[106:107]", "v[106:107]", TH, "v[106:107]")
#define K2_FAST(A0, A1, A2, A3, B0, B1, B2, B3, HAL, HAH, HBL, HBH, ADD)          \
        K2_RF(V2(B0, B1), V2(B2, B3), HBL, HBH) ADD K2_RF(V2(A0, A1), V2(A2, A3), HAL, HAH)
#define K2_SAFE(A0, A1, A2, A3, B0, B1, B2, B3, HAL, HAH, HBL, HBH, ADD)          \
        K2_ADD1(V2(B0, B1)) K2_ADD1(V2(B2, B3)) K2_ADD1(V2(A0, A1)) K2_ADD1(V2(A2, A3)) \
        ADD                                                                       \
        K2_RCP4(A0, A1, A2, A3) K2_RCP4(B0, B1, B2, B3)                           \
        K2_FMA("v[22:23]", V2(A0, A1), HAL, "v[22:23]") K2_FMA("v[24:25]", V2(A2, A3), HAH, "v[24:25]") \
        K2_FMA("v[22:23]", V2(B0, B1), HBL, "v[22:23]") K2_FMA("v[24:25]", V2(B2, B3), HBH, "v[24:25]")
#define K2_YES(x) x
#define K2_NO(x) ""
#define K2_PAIR(L, MATH, LD, VW, A0, A1, A2, A3, B0, B1, B2, B3, NA, NB_, H0, H1, H2, H3, H4, H5, H6, H7, DNA, DNB, EK, ERD, AL, AH, ADD, ARD) \
        "s_sub_u32 s66, s66, 1\n\t"                                               \
        "s_cbranch_scc1 .Lsw" #L "_%=\n"                                          \
        ".Lrt" #L "_%=:\n\t"                                                      \
        LD("s_waitcnt lgkmcnt(2)\n\t")                                            \
        "v_mfma_f32_16x16x32_bf16 " NA ", " EK ", v[14:17], v[18:21]\n\t"         \
        K2_EXP2(B0, B1)                                                           \
        "v_mfma_f32_16x16x32_bf16 " NB_ ", " EK ", v[12:15], v[18:21]\n\t"        \
        ERD                                                                       \
        K2_EXP2(B2, B3)                                                           \
        "s_waitcnt " VW "\n\t"                                                    \
        "v_mfma_f32_16x16x32_bf16 " NA ", v[8:11], " DNA ", " NA "\n\t"           \
        K2_EXP2(A0, A1)                                                           \
        "v_mfma_f32_16x16x32_bf16 " NB_ ", v[8:11], " DNB ", " NB_ "\n\t"         \
        K2_EXP2(A2, A3)                                                           \
        MATH(A0, A1, A2, A3, B0, B1, B2, B3, V2(H0, H1), V2(H2, H3), V2(H4, H5), V2(H6, H7), LD(ADD)) \
        LD("buffer_load_dwordx4 " V2(H0, H3) ", " AL ", %[t1r], 0 offen\n\t")     \
        LD("buffer_load_dwordx4 " V2(H4, H7) ", " AH ", %[t1r], 0 offen\n\t")     \
        LD(ARD)
#define K2_BODY(L, MATH, LD, W0, W1, W2, W3)                                      \
        K2_PAIR(L##0, MATH, LD, W0, 30, 31, 32, 33, 34, 35, 36, 37, "v[96:99]", "v[100:103]", 64, 65, 66, 67, 68, 69, 70, 71,   \
                "v[72:75]", "v[76:79]", "v[42:45]", K2_EB(48), "v46", "v47", K2_ADDA, K2_AA(48))                                \
        K2_PAIR(L##1, MATH, LD, W1, 96, 97, 98, 99, 100, 101, 102, 103, "v[30:33]", "v[34:37]", 72, 73, 74, 75, 76, 77, 78, 79, \
                "v[80:83]", "v[84:87]", "v[38:41]", LD(K2_EA(64)), "v48", "v49", K2_ADDB, K2_AB(56))                            \
        K2_PAIR(L##2, MATH, LD, W2, 30, 31, 32, 33, 34, 35, 36, 37, "v[96:99]", "v[100:103]", 80, 81, 82, 83, 84, 85, 86, 87,   \
                "v[88:91]", "v[92:95]", "v[42:45]", LD(K2_EB(80)), "v46", "v47", K2_ADDA, K2_AA(64))                            \
        K2_PAIR(L##3, MATH, LD, W3, 96, 97, 98, 99, 100, 101, 102, 103, "v[30:33]", "v[34:37]", 88, 89, 90, 91, 92, 93, 94, 95, \
                "v[64:67]", "v[68:71]", "v[38:41]", LD(K2_EA(96)), "v48", "v49", K2_ADDB, K2_AB(72))
#define K2_LOOP(L, MATH)                                                          \
        ".Lloop" #L "_%=:\n\t"                                                    \
        K2_BODY(L, MATH, K2_YES, "vmcnt(4)", "vmcnt(4)", "vmcnt(4)", "vmcnt(4)")  \
        "v_add_u32 v53, 64, v53\n\t"                                              \
        "v_add_u32 v52, 32, v52\n\t"                                              \
        BLK_STAMP                                                                 \
        "s_cmp_eq_u32 s71, 0\n\t"                                                 \
        "s_cbranch_scc1 .Lt2d" #L "_%=\n\t"                                       \
        "s_mov_b32 s71, 0\n\t"                                                    \
        K2_NEXT_T2                                                                \
        ".Lt2d" #L "_%=:\n\t"                                                     \
          \
        "s_sub_u32 s65, s65, 8\n\t"                                               \
        "s_cmp_gt_i32 s65, %c[p1]\n\t"                                            \
        "s_cbranch_scc1 .Lloop" #L "_%=\n\t"                                      \
        "s_setprio 1\n\t"                                                         \
        "s_cmp_gt_i32 s65, 8\n\t"                                                 \
        "s_cbranch_scc1 .Lloop" #L "_%=\n\t"                                      \
        "s_setprio 0\n\t"                                                         \
          \
        "s_waitcnt lgkmcnt(0)\n\t"                                                \
        K2_BODY(L##t, MATH, K2_NO, "vmcnt(4)", "vmcnt(2) lgkmcnt(0)", "vmcnt(0)", "vmcnt(0)") \
        BLK_STAMP                                                                 \
        "s_branch .Lflush_%=\n"                                                   \
        K2_SW(L##0) K2_SW(L##1) K2_SW(L##2) K2_SW(L##3) K2_SW(L##t0) K2_SW(L##t1) K2_SW(L##t2) K2_SW(L##t3)
#define K2_SW(K) ".Lsw" #K "_%=:\n\ts_swappc_b64 s[62:63], s[60:61]\n\ts_branch .Lrt" #K "_%=\n"
#define K2_ADDA "v_pk_add_f32 v[46:47], v[46:47], v[50:51]\n\t"
#define K2_ADDB "v_pk_add_f32 v[48:49], v[48:49], v[50:51]\n\t"
#define K2_EA(O) "ds_read_b128 v[38:41], v53 offset:" #O "\n\t"
#define K2_EB(O) "ds_read_b128 v[42:45], v53 offset:" #O "\n\t"
#define K2_AA(O) "ds_read_b64 v[46:47], v52 offset:" #O "\n\t"
#define K2_AB(O) "ds_read_b64 v[48:49], v52 offset:" #O "\n\t"
#define K2_NEXT_T2                                                                \
        "v_and_b32 v57, 0xff, v54\n\t"                                            \
        "v_lshrrev_b32 v54, 8, v54\n\t"                                           \
        "v_lshl_add_u32 v58, v57, 8, v50\n\t"                                     \
        "v_lshl_add_u32 v59, v57, 9, v7\n\t"                                      \
        "buffer_load_dwordx4 v[26:29], v58, %[t2r], 0 offen\n\t"
#define K2_CIN  "v_lshlrev_b32 v18, 16, v26\n\tv_lshlrev_b32 v19, 16, v27\n\tv_lshlrev_b32 v20, 16, v28\n\tv_lshlrev_b32 v21, 16, v29\n\t"
#define K2_ACC  "v_mov_b64 v[22:23], v[26:27]\n\tv_mov_b64 v[24:25], v[28:29]\n\tv_mov_b32 v56, v59\n\t"    \
                "v_mov_b64 v[104:105], s[72:73]\n\tv_mov_b64 v[106:107], s[72:73]\n\t"
#define K2_LD(D0, D3, A) "buffer_load_dwordx4 v[" #D0 ":" #D3 "], v" #A ", %[t1r], 0 offen\n\t"
    asm volatile(
        "s_cmp_lg_u32 %[slow], 0\n\t"
        "s_cbranch_scc1 .Ldone_%=\n\t"
        "s_mov_b64 s[74:75], %c[safe0]\n\t"
        "s_mov_b32 s70, 0\n"
        ".Lstart_%=:\n\t"
        "s_waitcnt lgkmcnt(0)\n\t"
        "ds_read_b128 v[30:33], %[vop]\n\t"
        "ds_read_b128 v[34:37], %[vop] offset:16\n\t"
        "v_mov_b32 v54, %[ordb]\n\t"
        "v_mov_b32 v53, %[vep]\n\t"
        "v_mov_b32 v52, %[vop]\n\t"
        "s_mov_b32 s64, %[lens]\n\t"
        "s_mov_b32 s65, %[tot]\n\t"
        K2_NEXT_T2
        "s_getpc_b64 s[60:61]\n"
        ".Lpc_%=:\n\t"
        "s_add_u32 s60, s60, .Lhnd_%=-.Lpc_%=\n\t"
        "s_addc_u32 s61, s61, 0\n\t"
        "s_and_b32 s66, s64, 0xff\n\t"
        "s_sub_u32 s66, s66, 1\n\t"
        "s_mov_b32 s67, 0\n\t"
        "s_mov_b32 s68, 0xbfb8aa3b\n\t"
        "s_mov_b32 s72, 1.0\n\t"
        "s_mov_b32 s73, 1.0\n\t"
        "s_mov_b32 s69, 0x7b800000\n\t"
        "v_mov_b32 v12, 0\n\t"
        "v_mov_b32 v13, 0\n\t"
        "v_mov_b32 v16, 0\n\t"
        "v_mov_b32 v17, 0\n\t"
        "s_waitcnt lgkmcnt(0)\n\t"
        "v_pk_add_f32 v[30:31], v[30:31], v[50:51]\n\t"
        "v_pk_add_f32 v[32:33], v[32:33], v[50:51]\n\t"
        "v_pk_add_f32 v[34:35], v[34:35], v[50:51]\n\t"
        "v_pk_add_f32 v[36:37], v[36:37], v[50:51]\n\t"
        K2_LD(64, 67, 30) K2_LD(68, 71, 31) K2_LD(72, 75, 32) K2_LD(76, 79, 33)
        "s_cmp_lg_u32 s70, 0\n\t"
        "s_cbranch_scc1 .Lnoe_%=\n\t"
        "s_waitcnt vmcnt(5)\n\t"
        "s_mov_b64 exec, %[m0]\n\tds_write_b64 %[q0], v[108:109] offset:%c[evo]\n\t"
        "s_mov_b64 exec, %[m1]\n\tds_write_b64 %[q1], v[110:111] offset:%c[evo]\n\t"
        "s_mov_b64 exec, %[m2]\n\tds_write_b64 %[q2], v[112:113] offset:%c[evo]\n\t"
        "s_mov_b64 exec, %[m3]\n\tds_write_b64 %[q3], v[114:115] offset:%c[evo]\n\t"
        "s_mov_b64 exec, -1\n\t"
        "s_waitcnt lgkmcnt(0)\n\t"
        "s_barrier\n"
        ".Lnoe_%=:\n\t"
        K2_EA(0) K2_EB(16) K2_AA(32) K2_AB(40)
        "s_setprio 2\n\t"
        "s_waitcnt vmcnt(4) lgkmcnt(3)\n\t"
        K2_CIN K2_ACC
        "v_mfma_f32_16x16x32_bf16 v[96:99], v[38:41], v[14:17], v[18:21]\n\t"
        "v_mfma_f32_16x16x32_bf16 v[100:103], v[38:41], v[12:15], v[18:21]\n\t"
        K2_EA(32)
        "s_waitcnt vmcnt(2)\n\t"
        K2_LD(80, 83, 34) K2_LD(84, 87, 35) K2_LD(88, 91, 36) K2_LD(92, 95, 37)
        "v_mfma_f32_16x16x32_bf16 v[30:33], v[8:11], v[64:67], v[96:99]\n\t"
        "v_mfma_f32_16x16x32_bf16 v[34:37], v[8:11], v[68:71], v[100:103]\n\t"
        "s_mov_b32 s71, 1\n\t"
        "s_cmp_ge_u32 s66, %c[t2d]\n\t"
        "s_cbranch_scc1 .Lt2l_%=\n\t"
        "s_mov_b32 s71, 0\n\t"
        K2_NEXT_T2
        ".Lt2l_%=:\n\t"
        BLK_INIT
        "s_cmp_lg_u64 s[74:75], 0\n\t"
        "s_nop 2\n\t"
        "s_cbranch_scc1 .Lloopsf_%=\n"
        K2_LOOP(fa, K2_FAST)
        ".Lhnd_%=:\n\t"
        "s_cmp_lg_u32 s67, 0\n\t"
        "s_cbranch_scc1 .Lflush_%=\n\t"
        K2_CIN
        "s_mov_b32 s66, 0\n\t"
        "s_mov_b32 s67, 1\n\t"
        "s_setpc_b64 s[62:63]\n"
        ".Lflush_%=:\n\t"
        "v_pk_fma_f32 v[60:61], v[22:23], 0, v[104:105] op_sel_hi:[1,0,1]\n\t"
        "v_pk_fma_f32 v[62:63], v[24:25], 0, v[106:107] op_sel_hi:[1,0,1]\n\t"
        K2_RCP4(104, 105, 106, 107)
        "v_pk_add_f32 v[60:61], v[60:61], v[62:63]\n\t"
        "v_pk_mul_f32 v[22:23], v[22:23], v[104:105]\n\t"
        "v_add_f32 v60, v60, v61\n\t"
        "v_pk_mul_f32 v[24:25], v[24:25], v[106:107]\n\t"
        "v_cmp_nge_f32 vcc, s69, v60\n\t"
        "s_andn2_b64 vcc, vcc, s[74:75]\n\t"
        "s_cbranch_scc1 .Lredo_%=\n\t"
        "v_pk_mul_f32 v[60:61], v[22:23], s[68:69] op_sel_hi:[1,0]\n\t"
        "v_pk_mul_f32 v[62:63], v[24:25], s[68:69] op_sel_hi:[1,0]\n\t"
        "v_exp_f32 v60, v60\n\t"
        "v_exp_f32 v61, v61\n\t"
        "v_exp_f32 v62, v62\n\t"
        "v_exp_f32 v63, v63\n\t"
        "v_pk_add_f32 v[60:61], v[60:61], 1.0 op_sel_hi:[1,0]\n\t"
        "v_pk_add_f32 v[62:63], v[62:63], 1.0 op_sel_hi:[1,0]\n\t"
        "v_rcp_f32 v60, v60\n\t"
        "v_rcp_f32 v61, v61\n\t"
        "v_rcp_f32 v62, v62\n\t"
        "v_rcp_f32 v63, v63\n\t"
        "v_pk_mul_f32 v[60:61], v[60:61], v[22:23]\n\t"
        "v_pk_mul_f32 v[62:63], v[62:63], v[24:25]\n\t"
        "v_cvt_pk_bf16_f32 v62, v60, v62\n\t"
        "v_cvt_pk_bf16_f32 v63, v61, v63\n\t"
        "s_mov_b32 vcc_lo, 0x55555555\n\t"
        "s_mov_b32 vcc_hi, 0x55555555\n\t"
        "v_cndmask_b32_dpp v60, v63, v62, vcc quad_perm:[1,0,3,2] row_mask:0xf bank_mask:0xf\n\t"
        "s_not_b64 vcc, vcc\n\t"
        "v_cndmask_b32_dpp v61, v62, v63, vcc quad_perm:[1,0,3,2] row_mask:0xf bank_mask:0xf\n\t"
        "s_lshr_b32 s64, s64, 8\n\t"
        "buffer_store_dwordx2 v[60:61], v56, %[outr], 0 offen" EXP_OUTF "\n\t"
        "s_and_b32 s66, s64, 0xff\n\t"
        "s_cbranch_scc0 .Ldone_%=\n\t"
        K2_ACC
        K2_NEXT_T2
        "s_sub_u32 s66, s66, 2\n\t"
        "s_mov_b32 s67, 0\n\t"
        "s_setpc_b64 s[62:63]\n"
        ".Legc_code_end:\n"
        K2_LOOP(sf, K2_SAFE)
        ".Lredo_%=:\n\t"
        "s_waitcnt vmcnt(0)\n\t"
        "s_mov_b64 s[74:75], -1\n\t"
        "s_mov_b32 s70, 1\n\t"
        "s_branch .Lstart_%=\n"
        ".Ldone_%=:\n\t"
        "s_waitcnt vmcnt(0) lgkmcnt(0)\n\t"
        BLK_LAST
        : BLK_OUT
        : [p1] "i"(EXP_P1), [t2d] "i"(EXP_T2D), [slow] "s"(slow), [safe0] "i"(EXP_SAFE ? -1 : 0), [lens] "s"(lens), [tot] "s"(tot), [vop] "v"(vop), [vep] "v"(vep),
          [ordb] "v"(ordb), [evo] "i"(K2_EV), [m0] "s"(vm[0]), [m1] "s"(vm[1]), [m2] "s"(vm[2]), [m3] "s"(vm[3]),
          [q0] "v"(p8[0]), [q1] "v"(p8[1]), [q2] "v"(p8[2]), [q3] "v"(p8[3]), "{v[108:111]}"(re[0]), "{v[112:115]}"(re[1]), [t1r] "s"(t1r), [t2r] "s"(t2r), [outr] "s"(outr), "{v[8:11]}"(asel), "{v[14:15]}"(cfv), "{v[50:51]}"(lo16),
          "{v7}"(olane)
        : "v12", "v13", "v16", "v17", "v18", "v19", "v20", "v21", "v22", "v23", "v24", "v25", "v26", "v27", "v28", "v29",
          "v30", "v31", "v32", "v33", "v34", "v35", "v36", "v37", "v38", "v39", "v40", "v41", "v42", "v43", "v44", "v45",
          "v46", "v47", "v48", "v49", "v52", "v53", "v54", "v56", "v57", "v58", "v59", "v60", "v61", "v62", "v63",
          "v64", "v65", "v66", "v67", "v68", "v69", "v70", "v71", "v72", "v73", "v74", "v75", "v76", "v77", "v78", "v79",
          "v80", "v81", "v82", "v83", "v84", "v85", "v86", "v87", "v88", "v89", "v90", "v91", "v92", "v93", "v94", "v95",
          "v96", "v97", "v98", "v99", "v100", "v101", "v102", "v103", "v104", "v105", "v106", "v107",
          "s60", "s61", "s62", "s63", "s64", "s65", "s66", "s67", "s68", "s69", "s70", "s71", "s72", "s73", "s74", "s75", BLK_CLOB
          "vcc", "scc", "memory");
    BLK_FLUSH;
    STAMP(1, 5);
    STAMP_FLUSH(1, 5);
    WSTAMP(0, stv[3]);
    WSTAMP(1, stv[5]);
    WSTAMP(2, tsteps);
    WSTAMP(3, (unsigned long long)__builtin_amdgcn_s_getreg(63492) | ((unsigned long long)__builtin_amdgcn_s_getreg(63508) << 32));
    if (slow) k2_slow(smem, T1, T2, gw, ea, src, dst, out, N, E, s, node0);
}

}

extern "C" void kernel_launch(void* const* d_in, const int* in_sizes, int n_in,
                              void* d_out, int out_size, void* d_ws, size_t ws_size,
                              hipStream_t stream) {
    const uint16_t* x  = (const uint16_t*)d_in[0];
    const uint16_t* ea = (const uint16_t*)d_in[1];
    const int* src     = (const int*)d_in[2];
    const int* dst     = (const int*)d_in[3];
    const uint16_t* uw = (const uint16_t*)d_in[4];
    const uint16_t* vw = (const uint16_t*)d_in[5];
    const uint16_t* gw = (const uint16_t*)d_in[6];
    const int N = in_sizes[0] / D;
    const int E = in_sizes[2];
    const int nrb   = (N + BM - 1) / BM;
    const int nbins = (N + NBN - 1) / NBN;
    int nchunks = 40;
    const int slots = nrb + nchunks / 8;
    const int ec = ((E + nchunks - 1) / nchunks + 3) & ~3;
    const int force_slow = (nbins > BINMAX || ec > RMAX * 2048) ? 1 : 0;
    if (force_slow) nchunks = 0;

    char* ws = (char*)d_ws;
    const size_t tb = ((size_t)NS * N * FS * 4 + 255) & ~(size_t)255;
    unsigned* T1 = (unsigned*)ws;
    unsigned* T2 = (unsigned*)(ws + tb);
    unsigned* recs = (unsigned*)(ws + 2 * tb);
    unsigned* tab = (unsigned*)(ws + 2 * tb + (((size_t)E * 12 + 16 + 255) & ~(size_t)255));

    (void)hipFuncSetAttribute((const void*)egc_k1, hipFuncAttributeMaxDynamicSharedMemorySize, K1_LDS);
    egc_k1<<<slots * 8, 512, K1_LDS, stream>>>(x, ea, src, dst, uw, vw, gw, T1, T2, recs, tab,
                                                 N, E, nrb, nbins, ec, nchunks, (N + nrb - 1) / nrb, NBK * 65536 / N);
    egc_k2<<<((nbins + 1) / 2) * 8, 512, 0, stream>>>(T1, T2, recs, tab, gw, ea, src, dst, (uint16_t*)d_out,
                                                      N, E, nchunks, nbins, ec, force_slow);
}
```

```cpp
#include <hip/hip_runtime.h>
#include <hip/hip_bf16.h>
#include <stdint.h>

typedef float    f32x4 __attribute__((ext_vector_type(4)));
typedef float    f32x2 __attribute__((ext_vector_type(2)));
typedef short    s16x4 __attribute__((ext_vector_type(4)));
typedef __bf16   bf16x8 __attribute__((ext_vector_type(8)));
typedef __bf16   bf16x2 __attribute__((ext_vector_type(2)));
typedef unsigned u32x4 __attribute__((ext_vector_type(4)));
typedef unsigned u32x2 __attribute__((ext_vector_type(2)));
typedef __amdgpu_buffer_rsrc_t rsrc_t;
#define LDSP __attribute__((address_space(3)))

namespace {

constexpr int D     = 256;
constexpr int FS    = 64;
constexpr int NS    = 4;
constexpr int BM    = 384;
constexpr int NB    = 80;
constexpr int NBN   = 79;
constexpr int NCH   = 40;
constexpr int TSTR  = 64;
constexpr int RMAX  = 2;
constexpr int BINMAX = 256;
constexpr int SLOTS = 8;
constexpr int TPC   = 12;
constexpr int NBK   = 4;
constexpr int LSTR  = 76;
constexpr int MAXT  = 62;
constexpr unsigned MINL = 8;
constexpr float NLOG2E = -1.4426950408889634f;
constexpr unsigned SENT = 0x00fff000u;
constexpr unsigned OOB  = 0x7ffffff0u;
constexpr int AUX_WT = 16;

__device__ __forceinline__ rsrc_t mk_rsrc(const void* p, unsigned bytes) {
    return __builtin_amdgcn_make_buffer_rsrc((void*)p, (short)0, (int)bytes, 0x00020000);
}
__device__ __forceinline__ void dma16(rsrc_t r, unsigned voff, unsigned soff, unsigned lds) {
    asm volatile("s_mov_b32 m0, %0\n\ts_nop 0\n\tbuffer_load_dwordx4 %1, %2, %3 offen lds"
                 :: "s"(lds), "v"(voff), "s"(r), "s"(soff) : "m0", "memory");
}
#define WAIT_V(n) asm volatile("s_waitcnt vmcnt(%0)" :: "i"(n) : "memory")
#define WAIT_L0() asm volatile("s_waitcnt lgkmcnt(0)" ::: "memory")
#define BAR() __builtin_amdgcn_s_barrier()

__device__ __forceinline__ unsigned wave_scan(unsigned v) {
    v += __builtin_amdgcn_update_dpp(0u, v, 0x111, 0xf, 0xf, false);
    v += __builtin_amdgcn_update_dpp(0u, v, 0x112, 0xf, 0xf, false);
    v += __builtin_amdgcn_update_dpp(0u, v, 0x114, 0xf, 0xf, false);
    v += __builtin_amdgcn_update_dpp(0u, v, 0x118, 0xf, 0xf, false);
    v += __builtin_amdgcn_update_dpp(0u, v, 0x142, 0xa, 0xf, false);
    v += __builtin_amdgcn_update_dpp(0u, v, 0x143, 0xc, 0xf, false);
    return v;
}
__device__ __forceinline__ unsigned pack_bf16(float lo, float hi) {
    f32x2 p = {lo, hi};
    return __builtin_bit_cast(unsigned, __builtin_convertvector(p, bf16x2));
}
__device__ __forceinline__ float bf_lo(unsigned v) { return __builtin_bit_cast(float, v << 16); }
__device__ __forceinline__ float bf_hi(unsigned v) { return __builtin_bit_cast(float, v & 0xffff0000u); }

__device__ __forceinline__ unsigned pack_t1(float xb, float hv) {
    asm("" : "+v"(xb), "+v"(hv));
    const float p = xb * NLOG2E;
    unsigned P;
    asm("v_cvt_pk_bf16_f32 %0, %1, -%1" : "=v"(P) : "v"(p));
    float m = __builtin_fabsf(hv) + 1.17549435e-38f;
    asm("" : "+v"(m));
    const unsigned t = __builtin_bit_cast(unsigned, m) - (unsigned)((int)P >> 16);
    unsigned X = (t & 0xffff0000u) | (P & 0xffffu);
    asm("v_and_or_b32 %0, %1, %2, %0" : "+v"(X) : "v"(hv), "s"(0x80000000u));
    return X;
}

constexpr int K1_W   = 98304;
constexpr int K1_LDS = 163840;
__host__ __device__ constexpr int K1_R(int r) { return r == 5 ? 0 : r * 32768; }

__device__ __forceinline__ void k1_gemm(char* smem, const unsigned lds0, const int lane, const int w, const unsigned g0,
                                        const unsigned xvo, const rsrc_t xr, const rsrc_t tr) {
    const int g = lane >> 4, n = lane & 15;
    const int fh = w & 1, rg = w >> 1;
    const unsigned ldw = lds0 + w * 1024;
    unsigned gr[6];
#pragma unroll
    for (int r = 0; r < 6; ++r) gr[r] = (g0 >> (4 * r)) & 15u;
#define K1_LDX(r, i) dma16(xr, xvo, gr[r] * 32768 + (i) * 8192, ldw + K1_R(r) + (i) * 8192)

    const f32x4 zero4 = {0.f, 0.f, 0.f, 0.f};
    bf16x8 wf[8][4];
    bf16x8 xq[8];
    f32x4 acc[2][4];
    const unsigned fr = ((n >> 3) & 1) | ((g & 1) << 1);
    const char* wp0 = smem + K1_W + (8 * g + (n >> 2)) * 128 + (((2 * fh) ^ fr) << 5) + 8 * (n & 3);
    const char* wp1 = smem + K1_W + (8 * g + (n >> 2)) * 128 + (((2 * fh + 1) ^ fr) << 5) + 8 * (n & 3);
    unsigned xa[3][4];
#pragma unroll
    for (int k = 0; k < 4; ++k) {
        xa[0][k] = lds0 + n * 512 + rg * 8192 + (((4 * k + g) ^ n) << 4);
        xa[1][k] = xa[0][k] + 65536; xa[2][k] = xa[0][k] + 131072;
    }
    const unsigned ovo = g * 1024 + fh * 128 + n * 8 + rg * 4096;
#define K1_RDW(ks) do { _Pragma("unroll") for (int mf = 0; mf < 4; ++mf) {                                   \
        const char* wp = ((mf & 1) ? wp1 : wp0) + (ks) * 8192 + (mf >> 1) * 4096;                           \
        u32x2 l2 = __builtin_bit_cast(u32x2, __builtin_amdgcn_ds_read_tr16_b64_v4i16((LDSP s16x4*)wp));      \
        u32x2 h2 = __builtin_bit_cast(u32x2, __builtin_amdgcn_ds_read_tr16_b64_v4i16((LDSP s16x4*)(wp + 512))); \
        wf[ks][mf] = __builtin_bit_cast(bf16x8, u32x4{l2.x, l2.y, h2.x, h2.y}); } } while (0)
#define K1_RDX(r, ks) xq[ks] = *(const LDSP bf16x8*)(size_t)(xa[K1_R(r) >> 16][(ks) & 3] + (K1_R(r) & 65535) + ((ks) >> 2) * 256)
#define K1_MM(p, ks) do { _Pragma("unroll") for (int m = 0; m < 4; ++m)                                      \
        acc[p][m] = __builtin_amdgcn_mfma_f32_16x16x32_bf16(xq[ks], wf[ks][m], (ks) ? acc[p][m] : zero4, 0, 0, 0); } while (0)
#define SB() __builtin_amdgcn_sched_barrier(0)

    WAIT_V(4); BAR();
#pragma unroll
    for (int ks = 0; ks < 6; ++ks) K1_RDW(ks);
#pragma unroll
    for (int i = 0; i < 4; ++i) K1_LDX(1, i);
    SB();
    WAIT_V(4); BAR();
#pragma unroll
    for (int ks = 0; ks < 8; ++ks) K1_RDX(0, ks);
    K1_RDW(6); K1_RDW(7);
    SB();
#pragma unroll
    for (int ks = 0; ks < 8; ++ks) {
        if (ks == 5) {
            WAIT_L0(); WAIT_V(4); BAR();
#pragma unroll
            for (int k = 0; k < 5; ++k) K1_RDX(1, k);
        }
        K1_MM(0, ks);
        if (ks >= 5) K1_RDX(1, ks);
        if (ks < 4) K1_LDX(2, ks);
        SB();
    }
#pragma unroll
    for (int r = 1; r < 6; ++r) {
        const int p = r & 1, q = p ^ 1;
        const unsigned vo = ovo + gr[r - 1] * 16384;
        u32x2 o;
#pragma unroll
        for (int ks = 0; ks < 8; ++ks) {
            if (r < 5 && ks == 4) {
                WAIT_V(r == 1 ? 6 : (r == 4 ? 4 : 8)); BAR();
#pragma unroll
                for (int k = 0; k < 4; ++k) K1_RDX(r + 1, k);
            }
            K1_MM(p, ks);
            if (r < 5 && ks >= 4) K1_RDX(r + 1, ks);
            o[ks & 1] = pack_t1(acc[q][ks & 1][ks >> 1], acc[q][2 + (ks & 1)][ks >> 1]);
            if (ks & 1) __builtin_amdgcn_raw_buffer_store_b64(o, tr, vo + (ks >> 1) * 256, 0, AUX_WT);
            if (r <= 3 && ks < 4) K1_LDX(r + 2, ks);
#pragma unroll
            for (int i = 0; i < 4; ++i) {
                __builtin_amdgcn_sched_group_barrier(0x008, 1, 0);
                __builtin_amdgcn_sched_group_barrier(0x002, 2, 0);
            }
            SB();
        }
    }
#pragma unroll
    for (int r = 0; r < 4; ++r) {
        const u32x2 o = {pack_t1(acc[1][0][r], acc[1][2][r]), pack_t1(acc[1][1][r], acc[1][3][r])};
        __builtin_amdgcn_raw_buffer_store_b64(o, tr, ovo + gr[5] * 16384 + r * 256, 0, AUX_WT);
    }
#undef K1_LDX
}

__global__ __launch_bounds__(512, 2)
void egc_k1(const uint16_t* __restrict__ x, const uint16_t* __restrict__ ea,
            const int* __restrict__ src, const int* __restrict__ dst,
            const uint16_t* __restrict__ uw, const uint16_t* __restrict__ vw,
            const uint16_t* __restrict__ gw,
            unsigned* __restrict__ T1, unsigned* __restrict__ T2,
            unsigned* __restrict__ recs, unsigned* __restrict__ tab,
            int N, int E, int nrb, int nbins, int ec, int nchunks, int rows, int bmul)
{
    extern __shared__ __attribute__((aligned(16))) char smem[];
    const int b   = blockIdx.x;
    const int slot = b >> 3;
    const int item = (b & 7) * nrb + slot;
    const int rb  = item >> 3;
    const int ct  = item & 7;
    const int tid = threadIdx.x;
    const int lane = tid & 63;
    const int w = __builtin_amdgcn_readfirstlane(tid >> 6);
    asm volatile(".Legc_code_begin:" :: "s"(x), "s"(ea), "s"(src), "s"(dst), "s"(uw), "s"(vw), "s"(gw), "s"(T1), "s"(T2), "s"(recs), "s"(tab),
                       "s"(N), "s"(E), "s"(nrb), "s"(nbins), "s"(ec), "s"(nchunks), "s"(rows), "s"(bmul));

    const int s = ct >> 1;
    const int row0 = rb * rows;
    const unsigned nr = (unsigned)max(min(rows, N - row0), 0);
    const unsigned lds0 = (unsigned)(size_t)(LDSP char*)smem;
    const rsrc_t xr = mk_rsrc(x + (size_t)row0 * D, nr * 512u);
    const unsigned rl = 2 * w + (lane >> 5);
    const unsigned xvo = rl * 512 + (((lane & 31) ^ rl) << 4);
    const unsigned g0 = (unsigned)(0x2154321543210ull >> (4 * ct)) & ~15u;
    {
        const uint16_t* wsel = (ct & 1) ? (w < 4 ? gw : uw) : (w < 4 ? gw + D * D : vw);
        const unsigned f = ((lane >> 4) & 1) | ((w & 1) << 1);
        const unsigned wvo = (lane >> 3) * 512 + s * 128 + ((((lane >> 1) & 3) ^ f) << 5) + (lane & 1) * 16 + (w & 3) * 4096;
        unsigned t0, t1;
#define K1_W1 "s_lshl_b32 %1, %0, 1\n\ts_add_u32 m0, %0, %[lw]\n\ts_add_u32 %0, %0, 0x2000\n\t"                  \
              "buffer_load_dwordx4 %[wvo], %[wr], %1 offen lds\n\ts_and_b32 %0, %0, 0xe000\n\t"
#define K1_X1(O) "s_add_u32 m0, %[lx], " #O "\n\ts_add_u32 %1, %[gx], " #O "\n\tbuffer_load_dwordx4 %[xvo], %[xr], %1 offen lds\n\t"
        asm volatile("s_cmp_ge_i32 %[slot], %[nrb]\n\t"
                     "s_cbranch_scc1 .Lk1skip_%=\n\t"
                     "s_mov_b32 %0, %[k0]\n\t"
                     K1_W1 K1_W1 K1_W1 K1_W1 K1_W1 K1_W1 K1_W1 K1_W1
                     "s_barrier\n\t"
                     K1_X1(0) K1_X1(0x2000) K1_X1(0x4000) K1_X1(0x6000)
                     ".Lk1skip_%=:"
                     : "=&s"(t0), "=&s"(t1)
                     : [slot] "s"(slot), [nrb] "s"(nrb), [k0] "s"((rb & 3) * 16384), [lw] "s"(lds0 + w * 1024 + K1_W),
                       [wvo] "v"(wvo), [wr] "s"(mk_rsrc(wsel, D * D * 2)), [lx] "s"(lds0 + w * 1024), [gx] "s"((g0 & 15u) * 32768),
                       [xvo] "v"(xvo), [xr] "s"(xr)
                     : "m0", "scc", "memory");
    }
    if (slot < nrb) {
        k1_gemm(smem, lds0, lane, w, g0, xvo, xr,
                mk_rsrc(((ct & 1) ? T2 : T1) + ((size_t)s * N + row0) * FS, nr * 256u));
        return;
    }
    {
        const int c = (slot - nrb) * 8 + (b & 7);
        unsigned cpf;
        {
            unsigned long long pc;
            unsigned len;
            unsigned back;
            asm volatile("s_getpc_b64 %0\n.Legc_code_here:\n\ts_mov_b32 %1, .Legc_code_end-.Legc_code_begin\n\t"
                         "s_mov_b32 %2, .Legc_code_here-.Legc_code_begin" : "=s"(pc), "=s"(len), "=s"(back));
            pc -= back;
            if (back > 65536u) len = 0;
            cpf = __builtin_amdgcn_raw_buffer_load_b32(mk_rsrc((const void*)pc, len > 65536u ? 0u : len), tid * 64, 0, 0);
        }
        if (c == 0 && tid < 256) {
            const int g = lane >> 4, n = lane & 15;
            const uint16_t* cw = gw + 2 * D * D + w * FS + (n >> 3) * 32 + (n & 7) * 2 + (g >> 1) + (g & 1) * 16;
            const u32x2 cf = {pack_bf16(bf_lo(cw[0]) * NLOG2E, bf_lo(cw[D]) * NLOG2E),
                              pack_bf16(bf_lo(cw[2 * D]) * NLOG2E, bf_lo(cw[3 * D]) * NLOG2E)};
            __builtin_amdgcn_raw_buffer_store_b64(cf, mk_rsrc(tab + BINMAX * TSTR, 2048), tid * 8, 0, AUX_WT);
        }
        if (c >= nchunks) { asm volatile("" :: "v"(cpf)); return; }
        unsigned* cw  = (unsigned*)smem;
        unsigned* wt  = cw + 8 * BINMAX;
        unsigned* rme = wt + 16;
        u32x2*    rev = (u32x2*)(rme + RMAX * 2048);
        const int e0 = c * ec;
        const int eb = ((tid >> 3) * NCH + c) * 32 + (tid & 7) * 4;
        constexpr int RS = 64 * NCH * 32;
        const rsrc_t dr = mk_rsrc(dst, E * 4u), sr = mk_rsrc(src, E * 4u), er = mk_rsrc(ea, E * 8u);
        u32x4 dv[RMAX], sv[RMAX], ev[RMAX][2];
#pragma unroll
        for (int r = 0; r < RMAX; ++r) dv[r] = __builtin_amdgcn_raw_buffer_load_b128(dr, eb * 4, r * RS * 4, 2);
        ((u32x4*)cw)[tid] = u32x4{0u, 0u, 0u, 0u};
#pragma unroll
        for (int r = 0; r < RMAX; ++r) {
            sv[r] = __builtin_amdgcn_raw_buffer_load_b128(sr, eb * 4, r * RS * 4, 2);
            ev[r][0] = __builtin_amdgcn_raw_buffer_load_b128(er, eb * 8, r * RS * 8, 2);
            ev[r][1] = __builtin_amdgcn_raw_buffer_load_b128(er, eb * 8 + 16, r * RS * 8, 2);
        }
        __builtin_amdgcn_sched_barrier(0);
        __syncthreads();
        asm volatile("" :: "v"(cpf));
        unsigned* cmine = cw + w * BINMAX;
        unsigned rk[RMAX][4], bn[RMAX][4];
#pragma unroll
        for (int r = 0; r < RMAX; ++r)
#pragma unroll
            for (int k = 0; k < 4; ++k) {
                bn[r][k] = __umul24(dv[r][k], 53093u) >> 22;
                rk[r][k] = 0;
                if (r * RS + eb + k < E) rk[r][k] = atomicAdd(&cmine[bn[r][k]], 1u);
            }
        __syncthreads();
        {
            unsigned pre[8], t = 0, inc = 0;
            if (tid < BINMAX) {
#pragma unroll
                for (int k = 0; k < 8; ++k) { pre[k] = t; t += cw[k * BINMAX + tid]; }
                inc = wave_scan(t);
                if (lane == 63) wt[w] = inc;
            }
            __syncthreads();
            if (tid < BINMAX) {
                const u32x4 w4 = *(const u32x4*)wt;
                const unsigned st = inc - t + (w > 0 ? w4[0] : 0u) + (w > 1 ? w4[1] : 0u) + (w > 2 ? w4[2] : 0u);
#pragma unroll
                for (int k = 0; k < 8; ++k) cw[k * BINMAX + tid] = st + pre[k];
                if (tid < nbins) __builtin_amdgcn_raw_buffer_store_b32(st | (t << 16), mk_rsrc(tab, BINMAX * TSTR * 4), (tid * TSTR + c) * 4, 0, AUX_WT);
            }
        }
        __syncthreads();
#pragma unroll
        for (int r = 0; r < RMAX; ++r)
#pragma unroll
            for (int k = 0; k < 4; ++k) rk[r][k] += cmine[bn[r][k]];
#pragma unroll
        for (int r = 0; r < RMAX; ++r)
#pragma unroll
            for (int k = 0; k < 4; ++k)
                if (r * RS + eb + k < E) {
                    rev[rk[r][k]] = u32x2{ev[r][k >> 1][(k & 1) * 2], ev[r][k >> 1][(k & 1) * 2 + 1]};
                    rme[rk[r][k]] = (sv[r][k] << 16) + (dv[r][k] - bn[r][k] * NBN + (__umul24(sv[r][k], bmul) >> 16) * NB) * 16u;
                }
        __syncthreads();
        const rsrc_t omr = mk_rsrc(recs + e0, ec * 4), oer = mk_rsrc(recs + NCH * ec + e0 * 2, ec * 8);
#pragma unroll
        for (int i = 0; i < RMAX * 3; ++i) {
            const u32x4 v = ((const u32x4*)rme)[tid + 512 * i];
            __builtin_amdgcn_raw_buffer_store_b128(v, i < RMAX ? omr : oer, tid * 16 + (i < RMAX ? i : i - RMAX) * 8192, 0, AUX_WT);
        }
    }
}

constexpr int K2_OFFS = 0;
constexpr int K2_EV   = K2_OFFS + 32 * LSTR * 4;
constexpr int K2_CNT  = K2_EV + 33 * LSTR * 8;
constexpr int K2_HIST = K2_CNT + NBK * NB * 16;
constexpr int K2_ORD  = K2_HIST + 256;
constexpr int K2_LEN  = K2_ORD + 128;
constexpr int K2_FLAG = K2_LEN + 32;
constexpr int K2_PF   = K2_FLAG + 16;
constexpr int K2_LDS  = K2_PF + 8192;
static_assert((K2_PF - K2_EV) % 16 == 0 && K2_EV % 16 == 0, "LDS map");

__device__ __forceinline__ u32x2 silu_pack(f32x4 y) {
    f32x4 o;
#pragma unroll
    for (int r = 0; r < 4; ++r) {
        float t = __builtin_amdgcn_exp2f(y[r] * NLOG2E);
        o[r] = y[r] * __builtin_amdgcn_rcpf(1.0f + t);
    }
    return u32x2{pack_bf16(o[0], o[1]), pack_bf16(o[2], o[3])};
}

__device__ __forceinline__ void k2_slow(char* smem, const unsigned* __restrict__ T1, const unsigned* __restrict__ T2,
                                        const uint16_t* __restrict__ gw, const uint16_t* __restrict__ ea,
                                        const int* __restrict__ src, const int* __restrict__ dst,
                                        uint16_t* __restrict__ out, int N, int E, int s, int node0) {
    float* accs = (float*)smem;
    unsigned* queue = (unsigned*)(smem + NB * FS * 4);
    static_assert(NB * FS * 4 + 2048 <= K2_CNT, "slow-path LDS");
    const unsigned* t2g = T2 + ((size_t)s * N + node0) * FS;
    const int tid = threadIdx.x, f = tid & 63, w = tid >> 6;
    __syncthreads();
    for (int i = tid; i < NB * FS; i += 512) accs[i] = 0.f;
    const uint16_t* cf = gw + (size_t)2 * D * D + s * FS + (f & 32) + (f & 1) * 16 + ((f & 31) >> 1);
    const float c0 = bf_lo(cf[0]) * NLOG2E, c1 = bf_lo(cf[D]) * NLOG2E;
    const float c2 = bf_lo(cf[2 * D]) * NLOG2E, c3 = bf_lo(cf[3 * D]) * NLOG2E;
#pragma unroll 1
    for (int eb = 0; eb < E; eb += 512) {
        __syncthreads();
        const int e = eb + tid;
        const int dn = (e < E) ? dst[e] - node0 : -1;
        queue[tid] = (dn >= 0 && dn < NBN) ? (unsigned)dn : 0xffffffffu;
        __syncthreads();
#pragma unroll 1
        for (int q0 = 0; q0 < 512; q0 += 64) {
            const unsigned qv = queue[q0 + f];
            unsigned long long m = __ballot(qv != 0xffffffffu && (qv & 7u) == (unsigned)w);
            while (m) {
                const int l = __builtin_ctzll(m);
                m &= m - 1;
                const int ee = eb + q0 + l;
                const int d2 = __builtin_amdgcn_readlane(qv, l);
                const unsigned tv = T1[((size_t)s * N + src[ee]) * FS + f];
                const u32x2 e2 = *(const u32x2*)(ea + (size_t)ee * 4);
                const float z = bf_lo(t2g[d2 * FS + f]) + bf_lo(tv) + bf_lo(e2.x) * c0 + bf_hi(e2.x) * c1
                                + bf_lo(e2.y) * c2 + bf_hi(e2.y) * c3;
                accs[d2 * FS + f] += __builtin_bit_cast(float, tv) * __builtin_amdgcn_rcpf(1.0f + __builtin_amdgcn_exp2f(z));
            }
        }
    }
    __syncthreads();
#pragma unroll 1
    for (int p = tid; p < NBN * 16; p += 512) {
        const int r = p >> 4, q4 = (p & 15) * 4;
        if (node0 + r < N) {
            f32x4 y;
#pragma unroll
            for (int k = 0; k < 4; ++k) {
                const int sl = r * FS + (q4 & 32) + ((q4 & 15) + k) * 2 + ((q4 >> 4) & 1);
                y[k] = accs[sl] + __builtin_bit_cast(float, t2g[sl]);
            }
            *(u32x2*)(out + (size_t)(node0 + r) * D + s * FS + q4) = silu_pack(y);
        }
    }
}

__global__ __launch_bounds__(512, 4)
void egc_k2(const unsigned* __restrict__ T1, const unsigned* __restrict__ T2,
            const unsigned* __restrict__ recs, const unsigned* __restrict__ tab,
            const uint16_t* __restrict__ gw, const uint16_t* __restrict__ ea,
            const int* __restrict__ src, const int* __restrict__ dst,
            uint16_t* __restrict__ out, int N, int E, int nchunks, int nbins, int ec, int force_slow)
{
    __shared__ __attribute__((aligned(16))) char smem[K2_LDS];
    unsigned* hist = (unsigned*)(smem + K2_HIST);
    unsigned* flag = (unsigned*)(smem + K2_FLAG);

    asm volatile("" :: "s"(T1), "s"(T2), "s"(recs), "s"(tab), "s"(out), "s"(N), "s"(nchunks),
                       "s"(nbins), "s"(ec), "s"(force_slow));
    const int b   = blockIdx.x;
    const int s   = (b & 7) >> 1;
    const int hb  = (nbins + 1) >> 1;
    const int bin = (b & 1) * hb + (b >> 3);
    const int tid = threadIdx.x;
    const int lane = tid & 63;
    const int w = __builtin_amdgcn_readfirstlane(tid >> 6);
    const int g = lane >> 4, n = lane & 15;
    const int node0 = bin * NBN;
    const unsigned lds0 = (unsigned)(size_t)(LDSP char*)smem;
    const rsrc_t t1r = mk_rsrc(T1 + (size_t)s * N * FS, (unsigned)N * 256u);
    __builtin_amdgcn_s_setprio(3);

    const unsigned c = __umul24(tid, 5462u) >> 16, j = tid - c * TPC;
    unsigned te;
    asm volatile("buffer_load_dword %0, %1, %2, 0 offen"
                 : "=&v"(te) : "v"(c * 4), "s"(mk_rsrc(tab + bin * TSTR, bin < nbins ? nchunks * 4 : 0)) : "memory");
    dma16(t1r, (b >> 3) * 8192u + tid * 16u, 0, lds0 + K2_PF + w * 1024);
    {
        const u32x4 sv = {SENT, SENT, SENT, SENT}, zv = {0u, 0u, 0u, 0u};
        u32x4* po = (u32x4*)(smem + K2_OFFS);
        constexpr int NO = 32 * LSTR / 4, NZ = (K2_PF - K2_EV) / 16;
        po[tid] = sv;
        if (tid < NO - 512) po[tid + 512] = sv;
        u32x4* pz = (u32x4*)(smem + K2_EV);
#pragma unroll
        for (int i = 0; i < NZ / 512; ++i) pz[tid + 512 * i] = zv;
        if (tid < NZ % 512) pz[tid + NZ / 512 * 512] = zv;
    }
    u32x4 asel;
#pragma unroll
    for (int k = 0; k < 4; ++k) asel[k] = (n == 4 * g + k) ? 0x3f80u : 0u;
    u32x2 lo16 = {(unsigned)n * 16u, (unsigned)n * 16u};
    unsigned vop = lds0 + K2_OFFS + (w * 4 + g) * (LSTR * 4);
    unsigned vep = lds0 + K2_EV + (((n & 3) == g) ? w * 4 + (n >> 2) : 32) * (LSTR * 8);
    unsigned olane = (n >> 3) * 64 + (n & 6) * 4 + (n & 1) * 32;
    unsigned j4 = 4 * j, rb0 = __umul24(c, ec) + j4;
    const rsrc_t mr = mk_rsrc(recs, NCH * 4u * ec), er = mk_rsrc(recs + NCH * ec, NCH * 8u * ec);
    asm volatile("s_waitcnt vmcnt(1)" : "+v"(te), "+v"(asel), "+v"(lo16), "+v"(vop), "+v"(vep), "+v"(olane), "+v"(j4), "+v"(rb0)
                 : "s"(mr), "s"(er) : "memory");
    const int nc = (int)(te >> 16) - (int)j4;
    u32x4 rm[2], re[4];
    const unsigned r0 = nc > 0 ? (rb0 + (te & 0xffffu)) * 4u : OOB / 2;
    asm volatile("buffer_load_dwordx4 %0, %1, %2, 0 offen" : "=&v"(rm[0]) : "v"(r0), "s"(mr) : "memory");
    __builtin_amdgcn_sched_barrier(0);
    unsigned long long vm[SLOTS];
#pragma unroll
    for (int i = 0; i < SLOTS; ++i) vm[i] = __builtin_amdgcn_ballot_w64(nc > (i >> 2) * (4 * TPC) + (i & 3));
    const bool big = vm[4] != 0;
    asm volatile("s_cmp_eq_u64 %3, 0\n\t"
                 "s_cbranch_scc1 .Lnb_%=\n\t"
                 "buffer_load_dwordx4 %0, %1, %2, 0 offen offset:192\n"
                 ".Lnb_%=:"
                 : "=&v"(rm[1]) : "v"(r0), "s"(mr), "s"(vm[4]) : "scc", "memory");
    if (bin >= nbins) { WAIT_V(0); return; }
    __syncthreads();
    if (nc > TPC * SLOTS || force_slow) flag[0] = 1u;
    asm volatile("s_waitcnt vmcnt(0)" : "+v"(rm[0]), "+v"(rm[1]) :: "memory");
    u32x2 cfv;
    asm volatile("s_cmp_eq_u64 %4, 0\n\t"
                 "s_cbranch_scc1 .Lnb_%=\n\t"
                 "buffer_load_dwordx4 %0, %2, %3, 0 offen offset:384\n\t"
                 "buffer_load_dwordx4 %1, %2, %3, 0 offen offset:400\n"
                 ".Lnb_%=:"
                 : "=&v"(re[2]), "=&v"(re[3]) : "v"(r0 * 2), "s"(er), "s"(vm[4]) : "scc", "memory");
    asm volatile("buffer_load_dwordx4 %0, %3, %5, 0 offen\n\t"
                 "buffer_load_dwordx4 %1, %3, %5, 0 offen offset:16\n\t"
                 "buffer_load_dwordx2 %2, %4, %6, 0 offen"
                 : "=&{v[108:111]}"(re[0]), "=&{v[112:115]}"(re[1]), "=&{v[14:15]}"(cfv)
                 : "v"(r0 * 2), "v"(lane * 8), "s"(er), "s"(mk_rsrc(tab + BINMAX * TSTR + s * 128, 512)) : "memory");
    unsigned ca[SLOTS], rk[SLOTS];
#define K2_CA(I) (lds0 + K2_CNT + (w & 4) * 2 + (rm[(I) >> 2][(I) & 3] & 0xffffu))
#pragma unroll
    for (int i = 0; i < 4; ++i) ca[i] = K2_CA(i);
#define K2_AT(R, A, M) "s_mov_b64 exec, %[" #M "]\n\tds_add_rtn_u32 %[" #R "], %[" #A "], %[one]\n\t"
#define K2_AT4(O) asm volatile(K2_AT(r0, a0, m0) K2_AT(r1, a1, m1) K2_AT(r2, a2, m2) K2_AT(r3, a3, m3)                   \
                 "s_mov_b64 exec, -1\n\ts_waitcnt lgkmcnt(0)"                                                            \
                 : [r0] "=&v"(rk[O]), [r1] "=&v"(rk[O + 1]), [r2] "=&v"(rk[O + 2]), [r3] "=&v"(rk[O + 3])                \
                 : [a0] "v"(ca[O]), [a1] "v"(ca[O + 1]), [a2] "v"(ca[O + 2]), [a3] "v"(ca[O + 3]),                       \
                   [m0] "s"(vm[O]), [m1] "s"(vm[O + 1]), [m2] "s"(vm[O + 2]), [m3] "s"(vm[O + 3]),                       \
                   [one] "v"(1u << (8 * (w & 3))) : "memory")
    K2_AT4(0);
    asm volatile("" : "=v"(rk[4]), "=v"(rk[5]), "=v"(rk[6]), "=v"(rk[7]));
    if (big) {
#pragma unroll
        for (int i = 4; i < SLOTS; ++i) ca[i] = K2_CA(i);
        K2_AT4(4);
    }
    __syncthreads();
    if (w >= 4) {
#pragma unroll
        for (int i = 0; i < 2; ++i)
            dma16(t1r, (b >> 3) * 4096u + (tid - 256) * 16u, hb * 4096u * (i + 2), lds0 + K2_PF + w * 1024);
    }

    unsigned pre[2 * NBK], deg = 0, key = 0, rank = 0;
    if (tid < NB) {
        u32x4 f4[NBK];
#pragma unroll
        for (int k = 0; k < NBK; ++k) f4[k] = *(const u32x4*)(smem + K2_CNT + (k * NB + tid) * 16);
#pragma unroll
        for (int k = 0; k < NBK; ++k) {
            pre[2 * k] = deg;
            pre[2 * k + 1] = __builtin_amdgcn_sad_u8(f4[k][0], 0u, deg);
            deg = __builtin_amdgcn_sad_u8(f4[k][2], 0u, pre[2 * k + 1]);
        }
        key = 63u - min(deg, 63u);
        rank = (atomicAdd(&hist[key], 1u << (16 * w)) >> (16 * w)) & 0xffffu;
    }
    {
        unsigned long long bad = 0;
#pragma unroll
        for (int i = 0; i < 4; ++i) {
            rk[i] = (rk[i] >> (8 * (w & 3))) & 255u;
            bad |= __builtin_amdgcn_ballot_w64(rk[i] > 63u) & vm[i];
        }
        if (big) {
#pragma unroll
            for (int i = 4; i < SLOTS; ++i) {
                rk[i] = (rk[i] >> (8 * (w & 3))) & 255u;
                bad |= __builtin_amdgcn_ballot_w64(rk[i] > 63u) & vm[i];
            }
        }
        if (bad) flag[0] = 1u;
    }
    __syncthreads();
    unsigned wv = 0, pos = 0;
    if (w < 2) {
        const unsigned hv = hist[lane], h1 = hv >> 16, ht = (hv & 0xffffu) + h1;
        const unsigned ex = wave_scan(ht) - (w ? h1 : ht);
        rank += __builtin_amdgcn_ds_bpermute(key * 4, ex);
        pos = rank >> 4;
        wv = ((rank >> 2) & 3u) ^ (pos ? 3u : 0u);
        if (pos >= 2) { wv += 4; pos -= 2; }
        if (tid < NB) {
            smem[K2_ORD + wv * 16 + (rank & 3u) * 4 + pos] = (char)tid;
            if ((rank & 3u) == 0) smem[K2_LEN + wv * 4 + pos] = (char)min(max((deg + 1u) & ~1u, MINL), 254u);
        }
    }
    __syncthreads();
    if (tid < NB) {
        const unsigned l4 = *(const unsigned*)(smem + K2_LEN + wv * 4);
        const unsigned first = __builtin_amdgcn_sad_u8(l4 & ((1u << (8 * pos)) - 1u), 0u, 0u);
        const unsigned base = (wv * 4 + (rank & 3u)) * LSTR + first;
        if (first + max(deg, MINL) > MAXT) flag[0] = 1u;
#pragma unroll
        for (int k = 0; k < NBK; ++k) {
            *(unsigned*)(smem + K2_CNT + (k * NB + tid) * 16 + 4) = base + pre[2 * k];
            *(unsigned*)(smem + K2_CNT + (k * NB + tid) * 16 + 12) = base + pre[2 * k + 1];
        }
    }
    unsigned lens = __builtin_amdgcn_readfirstlane(*(const unsigned*)(smem + K2_LEN + w * 4));
    unsigned ordb = *(const unsigned*)(smem + K2_ORD + w * 16 + g * 4);
    const rsrc_t t2r = mk_rsrc(T2 + ((size_t)s * N + node0) * FS, (unsigned)(N - node0) * 256u);
    dma16(t2r, (ordb & 255u) * 256u + lo16[0], 0, lds0 + K2_PF + w * 1024);
    __syncthreads();

    u32x2 cs[SLOTS];
#pragma unroll
    for (int i = 0; i < 4; ++i) cs[i] = *(const LDSP u32x2*)(size_t)ca[i];
    const unsigned slow = __builtin_amdgcn_readfirstlane(flag[0]);
#pragma unroll
    for (int i = 0; i < SLOTS; ++i) vm[i] = slow ? 0ull : vm[i];
    unsigned p8[4];
    {
        const unsigned lowm = (1u << (8 * (w & 3))) - 1u;
#pragma unroll
        for (int i = 0; i < 4; ++i) {
            const unsigned p = __builtin_amdgcn_sad_u8(cs[i][0] & lowm, 0u, rk[i]) + cs[i][1];
            asm volatile("s_mov_b64 exec, %2\n\tds_write_b32 %0, %1 offset:%c3\n\ts_mov_b64 exec, -1"
                         :: "v"(p * 4), "v"((rm[0][i] >> 16) << 8), "s"(vm[i]), "i"(K2_OFFS) : "memory");
            p8[i] = p * 8;
        }
        if (big) {
            asm volatile("s_waitcnt vmcnt(3)" : "+v"(re[2]), "+v"(re[3]) :: "memory");
#pragma unroll
            for (int i = 4; i < SLOTS; ++i) cs[i] = *(const LDSP u32x2*)(size_t)K2_CA(i);
#pragma unroll
            for (int i = 4; i < SLOTS; ++i) {
                const unsigned p = __builtin_amdgcn_sad_u8(cs[i][0] & lowm, 0u, rk[i]) + cs[i][1];
                const u32x2 e2 = {re[i >> 1][(i & 1) * 2], re[i >> 1][(i & 1) * 2 + 1]};
                asm volatile("s_mov_b64 exec, %4\n\tds_write_b32 %0, %1 offset:%c5\n\tds_write_b64 %2, %3 offset:%c6\n\ts_mov_b64 exec, -1"
                             :: "v"(p * 4), "v"((rm[1][i & 3] >> 16) << 8), "v"(p * 8), "v"(e2), "s"(vm[i]), "i"(K2_OFFS), "i"(K2_EV) : "memory");
            }
        }
    }
    const rsrc_t outr = mk_rsrc(out + (size_t)node0 * D + s * FS, (unsigned)min(N - node0, NBN) * 512u - s * 128);
    WAIT_L0();
    __syncthreads();
    unsigned tot = (lens & 255) + ((lens >> 8) & 255) + ((lens >> 16) & 255);
    lens >>= 1;

#define V2(a, b) "v[" #a ":" #b "]"
#define K2_EXP2(a, b) "v_exp_f32 v" #a ", v" #a "\n\tv_exp_f32 v" #b ", v" #b "\n\t"
#define K2_RCP4(a, b, c, d) "v_rcp_f32 v" #a ", v" #a "\n\tv_rcp_f32 v" #b ", v" #b "\n\tv_rcp_f32 v" #c ", v" #c "\n\tv_rcp_f32 v" #d ", v" #d "\n\t"
#define K2_ADD1(x) "v_pk_add_f32 " x ", " x ", 1.0 op_sel_hi:[1,0]\n\t"
#define K2_FMA(d, x, y, z) "v_pk_fma_f32 " d ", " x ", " y ", " z "\n\t"
#define K2_RF(TL, TH, HL, HH)                                                     \
        K2_FMA("v[22:23]", "v[22:23]", TL, "v[22:23]") K2_FMA("v[24:25]", "v[24:25]", TH, "v[24:25]")         \
        K2_FMA("v[22:23]", HL, "v[104:105]", "v[22:23]") K2_FMA("v[24:25]", HH, "v[106:107]", "v[24:25]")     \
        K2_FMA("v[104:105]", "v[104:105]", TL, "v[104:105]") K2_FMA("v[106:107]", "v[106:107]", TH, "v[106:107]")
#define K2_FAST(A0, A1, A2, A3, B0, B1, B2, B3, HAL, HAH, HBL, HBH, ADD)          \
        K2_RF(V2(B0, B1), V2(B2, B3), HBL, HBH) ADD K2_RF(V2(A0, A1), V2(A2, A3), HAL, HAH)
#define K2_SAFE(A0, A1, A2, A3, B0, B1, B2, B3, HAL, HAH, HBL, HBH, ADD)          \
        K2_ADD1(V2(B0, B1)) K2_ADD1(V2(B2, B3)) K2_ADD1(V2(A0, A1)) K2_ADD1(V2(A2, A3)) \
        ADD                                                                       \
        K2_RCP4(A0, A1, A2, A3) K2_RCP4(B0, B1, B2, B3)                           \
        K2_FMA("v[22:23]", V2(A0, A1), HAL, "v[22:23]") K2_FMA("v[24:25]", V2(A2, A3), HAH, "v[24:25]") \
        K2_FMA("v[22:23]", V2(B0, B1), HBL, "v[22:23]") K2_FMA("v[24:25]", V2(B2, B3), HBH, "v[24:25]")
#define K2_RAL "v[38:39]"
#define K2_RAH "v[40:41]"
#define K2_RBL "v[42:43]"
#define K2_RBH "v[44:45]"
#define K2_YES(x) x
#define K2_NO(x) ""
#define K2_PAIR(LB, MATH, LD, VW, A0, A1, A2, A3, B0, B1, B2, B3, NA, NB_, H0, H1, H2, H3, H4, H5, H6, H7, DNA, DNB, EK, ERD, AL, AH, ADD, ARD) \
        "s_sub_u32 s66, s66, 1\n\t"                                               \
        "s_cbranch_scc1 .Lsw" #LB "_%=\n"                                          \
        ".Lrt" #LB "_%=:\n\t"                                                      \
        LD("s_waitcnt lgkmcnt(2)\n\t")                                            \
        "v_mfma_f32_16x16x16_bf16 " NA ", " EK##L ", v[14:15], v[18:21]\n\t"      \
        K2_EXP2(B0, B1)                                                           \
        "v_mfma_f32_16x16x16_bf16 " NB_ ", " EK##H ", v[14:15], v[18:21]\n\t"     \
        ERD                                                                       \
        K2_EXP2(B2, B3)                                                           \
        "s_waitcnt " VW "\n\t"                                                    \
        "v_mfma_f32_16x16x32_bf16 " NA ", v[8:11], " DNA ", " NA "\n\t"           \
        K2_EXP2(A0, A1)                                                           \
        "v_mfma_f32_16x16x32_bf16 " NB_ ", v[8:11], " DNB ", " NB_ "\n\t"         \
        K2_EXP2(A2, A3)                                                           \
        MATH(A0, A1, A2, A3, B0, B1, B2, B3, V2(H0, H1), V2(H2, H3), V2(H4, H5), V2(H6, H7), LD(ADD)) \
        LD("buffer_load_dwordx4 " V2(H0, H3) ", " AL ", %[t1r], 0 offen\n\t")     \
        LD("buffer_load_dwordx4 " V2(H4, H7) ", " AH ", %[t1r], 0 offen\n\t")     \
        LD(ARD)
#define K2_BODY(L, MATH, LD, W0, W1, W2, W3)                                      \
        K2_PAIR(L##0, MATH, LD, W0, 30, 31, 32, 33, 34, 35, 36, 37, "v[96:99]", "v[100:103]", 64, 65, 66, 67, 68, 69, 70, 71,   \
                "v[72:75]", "v[76:79]", K2_RB, K2_EB(48), "v46", "v47", K2_ADDA, K2_AA(48))                                \
        K2_PAIR(L##1, MATH, LD, W1, 96, 97, 98, 99, 100, 101, 102, 103, "v[30:33]", "v[34:37]", 72, 73, 74, 75, 76, 77, 78, 79, \
                "v[80:83]", "v[84:87]", K2_RA, LD(K2_EA(64)), "v48", "v49", K2_ADDB, K2_AB(56))                            \
        K2_PAIR(L##2, MATH, LD, W2, 30, 31, 32, 33, 34, 35, 36, 37, "v[96:99]", "v[100:103]", 80, 81, 82, 83, 84, 85, 86, 87,   \
                "v[88:91]", "v[92:95]", K2_RB, LD(K2_EB(80)), "v46", "v47", K2_ADDA, K2_AA(64))                            \
        K2_PAIR(L##3, MATH, LD, W3, 96, 97, 98, 99, 100, 101, 102, 103, "v[30:33]", "v[34:37]", 88, 89, 90, 91, 92, 93, 94, 95, \
                "v[64:67]", "v[68:71]", K2_RA, LD(K2_EA(96)), "v48", "v49", K2_ADDB, K2_AB(72))
#define K2_LOOP(L, MATH)                                                          \
        ".Lloop" #L "_%=:\n\t"                                                    \
        K2_BODY(L, MATH, K2_YES, "vmcnt(4)", "vmcnt(4)", "vmcnt(4)", "vmcnt(4)")  \
        "v_add_u32 v53, 64, v53\n\t"                                              \
        "v_add_u32 v52, 32, v52\n\t"                                              \
        "s_cmp_eq_u32 s71, 0\n\t"                                                 \
        "s_cbranch_scc1 .Lt2d" #L "_%=\n\t"                                       \
        "s_mov_b32 s71, 0\n\t"                                                    \
        K2_NEXT_T2                                                                \
        ".Lt2d" #L "_%=:\n\t"                                                     \
          \
        "s_sub_u32 s65, s65, 8\n\t"                                               \
        "s_cmp_gt_i32 s65, 20\n\t"                                            \
        "s_cbranch_scc1 .Lloop" #L "_%=\n\t"                                      \
        "s_setprio 1\n\t"                                                         \
        "s_cmp_gt_i32 s65, 8\n\t"                                                 \
        "s_cbranch_scc1 .Lloop" #L "_%=\n\t"                                      \
        "s_setprio 0\n\t"                                                         \
          \
        "s_waitcnt lgkmcnt(0)\n\t"                                                \
        K2_BODY(L##t, MATH, K2_NO, "vmcnt(4)", "vmcnt(2) lgkmcnt(0)", "vmcnt(0)", "vmcnt(0)") \
        "s_branch .Lflush_%=\n"                                                   \
        K2_SW(L##0) K2_SW(L##1) K2_SW(L##2) K2_SW(L##3) K2_SW(L##t0) K2_SW(L##t1) K2_SW(L##t2) K2_SW(L##t3)
#define K2_SW(K) ".Lsw" #K "_%=:\n\ts_swappc_b64 s[62:63], s[60:61]\n\ts_branch .Lrt" #K "_%=\n"
#define K2_ADDA "v_pk_add_f32 v[46:47], v[46:47], v[50:51]\n\t"
#define K2_ADDB "v_pk_add_f32 v[48:49], v[48:49], v[50:51]\n\t"
#define K2_EA(O) "ds_read_b128 v[38:41], v53 offset:" #O "\n\t"
#define K2_EB(O) "ds_read_b128 v[42:45], v53 offset:" #O "\n\t"
#define K2_AA(O) "ds_read_b64 v[46:47], v52 offset:" #O "\n\t"
#define K2_AB(O) "ds_read_b64 v[48:49], v52 offset:" #O "\n\t"
#define K2_NEXT_T2                                                                \
        "v_and_b32 v57, 0xff, v54\n\t"                                            \
        "v_lshrrev_b32 v54, 8, v54\n\t"                                           \
        "v_lshl_add_u32 v58, v57, 8, v50\n\t"                                     \
        "v_lshl_add_u32 v59, v57, 9, v7\n\t"                                      \
        "buffer_load_dwordx4 v[26:29], v58, %[t2r], 0 offen\n\t"
#define K2_CIN  "v_lshlrev_b32 v18, 16, v26\n\tv_lshlrev_b32 v19, 16, v27\n\tv_lshlrev_b32 v20, 16, v28\n\tv_lshlrev_b32 v21, 16, v29\n\t"
#define K2_ACC  "v_mov_b64 v[22:23], v[26:27]\n\tv_mov_b64 v[24:25], v[28:29]\n\tv_mov_b32 v56, v59\n\t"    \
                "v_mov_b64 v[104:105], s[72:73]\n\tv_mov_b64 v[106:107], s[72:73]\n\t"
#define K2_LD(D0, D3, A) "buffer_load_dwordx4 v[" #D0 ":" #D3 "], v" #A ", %[t1r], 0 offen\n\t"
    asm volatile(
        "s_cmp_lg_u32 %[slow], 0\n\t"
        "s_cbranch_scc1 .Ldone_%=\n\t"
        "s_mov_b64 s[74:75], 0\n\t"
        "s_mov_b32 s70, 0\n"
        ".Lstart_%=:\n\t"
        "s_waitcnt lgkmcnt(0)\n\t"
        "ds_read_b128 v[30:33], %[vop]\n\t"
        "ds_read_b128 v[34:37], %[vop] offset:16\n\t"
        "v_mov_b32 v54, %[ordb]\n\t"
        "v_mov_b32 v53, %[vep]\n\t"
        "v_mov_b32 v52, %[vop]\n\t"
        "s_mov_b32 s64, %[lens]\n\t"
        "s_mov_b32 s65, %[tot]\n\t"
        K2_NEXT_T2
        "s_getpc_b64 s[60:61]\n"
        ".Lpc_%=:\n\t"
        "s_add_u32 s60, s60, .Lhnd_%=-.Lpc_%=\n\t"
        "s_addc_u32 s61, s61, 0\n\t"
        "s_and_b32 s66, s64, 0xff\n\t"
        "s_sub_u32 s66, s66, 1\n\t"
        "s_mov_b32 s67, 0\n\t"
        "s_mov_b32 s68, 0xbfb8aa3b\n\t"
        "s_mov_b32 s72, 1.0\n\t"
        "s_mov_b32 s73, 1.0\n\t"
        "s_mov_b32 s69, 0x7b800000\n\t"
        "s_waitcnt lgkmcnt(0)\n\t"
        "v_pk_add_f32 v[30:31], v[30:31], v[50:51]\n\t"
        "v_pk_add_f32 v[32:33], v[32:33], v[50:51]\n\t"
        "v_pk_add_f32 v[34:35], v[34:35], v[50:51]\n\t"
        "v_pk_add_f32 v[36:37], v[36:37], v[50:51]\n\t"
        K2_LD(64, 67, 30) K2_LD(68, 71, 31) K2_LD(72, 75, 32) K2_LD(76, 79, 33)
        "s_cmp_lg_u32 s70, 0\n\t"
        "s_cbranch_scc1 .Lnoe_%=\n\t"
        "s_waitcnt vmcnt(5)\n\t"
        "s_mov_b64 exec, %[m0]\n\tds_write_b64 %[q0], v[108:109] offset:%c[evo]\n\t"
        "s_mov_b64 exec, %[m1]\n\tds_write_b64 %[q1], v[110:111] offset:%c[evo]\n\t"
        "s_mov_b64 exec, %[m2]\n\tds_write_b64 %[q2], v[112:113] offset:%c[evo]\n\t"
        "s_mov_b64 exec, %[m3]\n\tds_write_b64 %[q3], v[114:115] offset:%c[evo]\n\t"
        "s_mov_b64 exec, -1\n\t"
        "s_waitcnt lgkmcnt(0)\n\t"
        "s_barrier\n"
        ".Lnoe_%=:\n\t"
        K2_EA(0) K2_EB(16) K2_AA(32) K2_AB(40)
        "s_setprio 2\n\t"
        "s_waitcnt vmcnt(4) lgkmcnt(3)\n\t"
        K2_CIN K2_ACC
        "v_mfma_f32_16x16x16_bf16 v[96:99], v[38:39], v[14:15], v[18:21]\n\t"
        "v_mfma_f32_16x16x16_bf16 v[100:103], v[40:41], v[14:15], v[18:21]\n\t"
        K2_EA(32)
        "s_waitcnt vmcnt(2)\n\t"
        K2_LD(80, 83, 34) K2_LD(84, 87, 35) K2_LD(88, 91, 36) K2_LD(92, 95, 37)
        "v_mfma_f32_16x16x32_bf16 v[30:33], v[8:11], v[64:67], v[96:99]\n\t"
        "v_mfma_f32_16x16x32_bf16 v[34:37], v[8:11], v[68:71], v[100:103]\n\t"
        "s_mov_b32 s71, 1\n\t"
        "s_cmp_ge_u32 s66, 7\n\t"
        "s_cbranch_scc1 .Lt2l_%=\n\t"
        "s_mov_b32 s71, 0\n\t"
        K2_NEXT_T2
        ".Lt2l_%=:\n\t"
        "s_cmp_lg_u64 s[74:75], 0\n\t"
        "s_nop 2\n\t"
        "s_cbranch_scc1 .Lloopsf_%=\n"
        K2_LOOP(fa, K2_FAST)
        ".Lhnd_%=:\n\t"
        "s_cmp_lg_u32 s67, 0\n\t"
        "s_cbranch_scc1 .Lflush_%=\n\t"
        K2_CIN
        "s_mov_b32 s66, 0\n\t"
        "s_mov_b32 s67, 1\n\t"
        "s_setpc_b64 s[62:63]\n"
        ".Lflush_%=:\n\t"
        "v_pk_fma_f32 v[60:61], v[22:23], 0, v[104:105] op_sel_hi:[1,0,1]\n\t"
        "v_pk_fma_f32 v[62:63], v[24:25], 0, v[106:107] op_sel_hi:[1,0,1]\n\t"
        K2_RCP4(104, 105, 106, 107)
        "v_pk_add_f32 v[60:61], v[60:61], v[62:63]\n\t"
        "v_pk_mul_f32 v[22:23], v[22:23], v[104:105]\n\t"
        "v_add_f32 v60, v60, v61\n\t"
        "v_pk_mul_f32 v[24:25], v[24:25], v[106:107]\n\t"
        "v_cmp_nge_f32 vcc, s69, v60\n\t"
        "s_andn2_b64 vcc, vcc, s[74:75]\n\t"
        "s_cbranch_scc1 .Lredo_%=\n\t"
        "v_pk_mul_f32 v[60:61], v[22:23], s[68:69] op_sel_hi:[1,0]\n\t"
        "v_pk_mul_f32 v[62:63], v[24:25], s[68:69] op_sel_hi:[1,0]\n\t"
        "v_exp_f32 v60, v60\n\t"
        "v_exp_f32 v61, v61\n\t"
        "v_exp_f32 v62, v62\n\t"
        "v_exp_f32 v63, v63\n\t"
        "v_pk_add_f32 v[60:61], v[60:61], 1.0 op_sel_hi:[1,0]\n\t"
        "v_pk_add_f32 v[62:63], v[62:63], 1.0 op_sel_hi:[1,0]\n\t"
        "v_rcp_f32 v60, v60\n\t"
        "v_rcp_f32 v61, v61\n\t"
        "v_rcp_f32 v62, v62\n\t"
        "v_rcp_f32 v63, v63\n\t"
        "v_pk_mul_f32 v[60:61], v[60:61], v[22:23]\n\t"
        "v_pk_mul_f32 v[62:63], v[62:63], v[24:25]\n\t"
        "v_cvt_pk_bf16_f32 v62, v60, v62\n\t"
        "v_cvt_pk_bf16_f32 v63, v61, v63\n\t"
        "s_mov_b32 vcc_lo, 0x55555555\n\t"
        "s_mov_b32 vcc_hi, 0x55555555\n\t"
        "v_cndmask_b32_dpp v60, v63, v62, vcc quad_perm:[1,0,3,2] row_mask:0xf bank_mask:0xf\n\t"
        "s_not_b64 vcc, vcc\n\t"
        "v_cndmask_b32_dpp v61, v62, v63, vcc quad_perm:[1,0,3,2] row_mask:0xf bank_mask:0xf\n\t"
        "s_lshr_b32 s64, s64, 8\n\t"
        "buffer_store_dwordx2 v[60:61], v56, %[outr], 0 offen sc1\n\t"
        "s_and_b32 s66, s64, 0xff\n\t"
        "s_cbranch_scc0 .Ldone_%=\n\t"
        K2_ACC
        K2_NEXT_T2
        "s_sub_u32 s66, s66, 2\n\t"
        "s_mov_b32 s67, 0\n\t"
        "s_setpc_b64 s[62:63]\n"
        ".Legc_code_end:\n"
        K2_LOOP(sf, K2_SAFE)
        ".Lredo_%=:\n\t"
        "s_waitcnt vmcnt(0)\n\t"
        "s_mov_b64 s[74:75], -1\n\t"
        "s_mov_b32 s70, 1\n\t"
        "s_branch .Lstart_%=\n"
        ".Ldone_%=:\n\t"
        "s_waitcnt vmcnt(0) lgkmcnt(0)\n\t"
        :
        : [slow] "s"(slow), [lens] "s"(lens), [tot] "s"(tot), [vop] "v"(vop), [vep] "v"(vep),
          [ordb] "v"(ordb), [evo] "i"(K2_EV), [m0] "s"(vm[0]), [m1] "s"(vm[1]), [m2] "s"(vm[2]), [m3] "s"(vm[3]),
          [q0] "v"(p8[0]), [q1] "v"(p8[1]), [q2] "v"(p8[2]), [q3] "v"(p8[3]), "{v[108:111]}"(re[0]), "{v[112:115]}"(re[1]), [t1r] "s"(t1r), [t2r] "s"(t2r), [outr] "s"(outr), "{v[8:11]}"(asel), "{v[14:15]}"(cfv), "{v[50:51]}"(lo16),
          "{v7}"(olane)
        : "v18", "v19", "v20", "v21", "v22", "v23", "v24", "v25", "v26", "v27", "v28", "v29",
          "v30", "v31", "v32", "v33", "v34", "v35", "v36", "v37", "v38", "v39", "v40", "v41", "v42", "v43", "v44", "v45",
          "v46", "v47", "v48", "v49", "v52", "v53", "v54", "v56", "v57", "v58", "v59", "v60", "v61", "v62", "v63",
          "v64", "v65", "v66", "v67", "v68", "v69", "v70", "v71", "v72", "v73", "v74", "v75", "v76", "v77", "v78", "v79",
          "v80", "v81", "v82", "v83", "v84", "v85", "v86", "v87", "v88", "v89", "v90", "v91", "v92", "v93", "v94", "v95",
          "v96", "v97", "v98", "v99", "v100", "v101", "v102", "v103", "v104", "v105", "v106", "v107",
          "s60", "s61", "s62", "s63", "s64", "s65", "s66", "s67", "s68", "s69", "s70", "s71", "s72", "s73", "s74", "s75",
          "vcc", "scc", "memory");
    if (slow) k2_slow(smem, T1, T2, gw, ea, src, dst, out, N, E, s, node0);
}

}

extern "C" void kernel_launch(void* const* d_in, const int* in_sizes, int n_in,
                              void* d_out, int out_size, void* d_ws, size_t ws_size,
                              hipStream_t stream) {
    const uint16_t* x  = (const uint16_t*)d_in[0];
    const uint16_t* ea = (const uint16_t*)d_in[1];
    const int* src     = (const int*)d_in[2];
    const int* dst     = (const int*)d_in[3];
    const uint16_t* uw = (const uint16_t*)d_in[4];
    const uint16_t* vw = (const uint16_t*)d_in[5];
    const uint16_t* gw = (const uint16_t*)d_in[6];
    const int N = in_sizes[0] / D;
    const int E = in_sizes[2];
    const int nrb   = (N + BM - 1) / BM;
    const int nbins = (N + NBN - 1) / NBN;
    int nchunks = NCH;
    const int slots = nrb + nchunks / 8;
    int ec = (E + NCH * 32 - 1) / (NCH * 32) * 32;
    const int force_slow = (nbins > BINMAX || ec > RMAX * 2048) ? 1 : 0;
    if (force_slow) { nchunks = 0; ec = 0; }

    char* ws = (char*)d_ws;
    const size_t tb = ((size_t)NS * N * FS * 4 + 255) & ~(size_t)255;
    unsigned* T1 = (unsigned*)ws;
    unsigned* T2 = (unsigned*)(ws + tb);
    unsigned* recs = (unsigned*)(ws + 2 * tb);
    unsigned* tab = (unsigned*)(ws + 2 * tb + (((size_t)NCH * ec * 12 + 255) & ~(size_t)255));

    (void)hipFuncSetAttribute((const void*)egc_k1, hipFuncAttributeMaxDynamicSharedMemorySize, K1_LDS);
    egc_k1<<<slots * 8, 512, K1_LDS, stream>>>(x, ea, src, dst, uw, vw, gw, T1, T2, recs, tab,
                                                 N, E, nrb, nbins, ec, nchunks, (N + nrb - 1) / nrb, NBK * 65536 / N);
    egc_k2<<<((nbins + 1) / 2) * 8, 512, 0, stream>>>(T1, T2, recs, tab, gw, ea, src, dst, (uint16_t*)d_out,
                                                      N, E, nchunks, nbins, ec, force_slow);
}
```

```cpp
#include <hip/hip_runtime.h>
#include <hip/hip_bf16.h>
#include <stdint.h>

typedef float    f32x4 __attribute__((ext_vector_type(4)));
typedef float    f32x2 __attribute__((ext_vector_type(2)));
typedef short    s16x4 __attribute__((ext_vector_type(4)));
typedef __bf16   bf16x8 __attribute__((ext_vector_type(8)));
typedef __bf16   bf16x2 __attribute__((ext_vector_type(2)));
typedef unsigned u32x4 __attribute__((ext_vector_type(4)));
typedef unsigned u32x2 __attribute__((ext_vector_type(2)));
typedef __amdgpu_buffer_rsrc_t rsrc_t;
#define LDSP __attribute__((address_space(3)))

namespace {

constexpr int D     = 256;
constexpr int FS    = 64;
constexpr int NS    = 4;
constexpr int BM    = 384;
constexpr int NB    = 80;
constexpr int NBN   = 79;
constexpr int NCH   = 40;
constexpr int TSTR  = 64;
constexpr int RMAX  = 2;
constexpr int BINMAX = 256;
constexpr int SLOTS = 8;
constexpr int TPC   = 12;
constexpr int NBK   = 4;
constexpr int LSTR  = 76;
constexpr int MAXT  = 62;
constexpr unsigned MINL = 8;
constexpr float NLOG2E = -1.4426950408889634f;
constexpr unsigned SENT = 0x00fff000u;
constexpr unsigned OOB  = 0x7ffffff0u;
constexpr int AUX_WT = 16;

__device__ __forceinline__ rsrc_t mk_rsrc(const void* p, unsigned bytes) {
    return __builtin_amdgcn_make_buffer_rsrc((void*)p, (short)0, (int)bytes, 0x00020000);
}
__device__ __forceinline__ void dma16(rsrc_t r, unsigned voff, unsigned soff, unsigned lds) {
    asm volatile("s_mov_b32 m0, %0\n\ts_nop 0\n\tbuffer_load_dwordx4 %1, %2, %3 offen lds"
                 :: "s"(lds), "v"(voff), "s"(r), "s"(soff) : "m0", "memory");
}
#define WAIT_V(n) asm volatile("s_waitcnt vmcnt(%0)" :: "i"(n) : "memory")
#define WAIT_L0() asm volatile("s_waitcnt lgkmcnt(0)" ::: "memory")
#define BAR() __builtin_amdgcn_s_barrier()

__device__ __forceinline__ unsigned wave_scan(unsigned v) {
    v += __builtin_amdgcn_update_dpp(0u, v, 0x111, 0xf, 0xf, false);
    v += __builtin_amdgcn_update_dpp(0u, v, 0x112, 0xf, 0xf, false);
    v += __builtin_amdgcn_update_dpp(0u, v, 0x114, 0xf, 0xf, false);
    v += __builtin_amdgcn_update_dpp(0u, v, 0x118, 0xf, 0xf, false);
    v += __builtin_amdgcn_update_dpp(0u, v, 0x142, 0xa, 0xf, false);
    v += __builtin_amdgcn_update_dpp(0u, v, 0x143, 0xc, 0xf, false);
    return v;
}
__device__ __forceinline__ unsigned pack_bf16(float lo, float hi) {
    f32x2 p = {lo, hi};
    return __builtin_bit_cast(unsigned, __builtin_convertvector(p, bf16x2));
}
__device__ __forceinline__ float bf_lo(unsigned v) { return __builtin_bit_cast(float, v << 16); }
__device__ __forceinline__ float bf_hi(unsigned v) { return __builtin_bit_cast(float, v & 0xffff0000u); }

__device__ __forceinline__ unsigned pack_t1(float xb, float hv) {
    asm("" : "+v"(xb), "+v"(hv));
    const float p = xb * NLOG2E;
    unsigned P;
    asm("v_cvt_pk_bf16_f32 %0, %1, -%1" : "=v"(P) : "v"(p));
    float m = __builtin_fabsf(hv) + 1.17549435e-38f;
    asm("" : "+v"(m));
    const unsigned t = __builtin_bit_cast(unsigned, m) - (unsigned)((int)P >> 16);
    unsigned X = (t & 0xffff0000u) | (P & 0xffffu);
    asm("v_and_or_b32 %0, %1, %2, %0" : "+v"(X) : "v"(hv), "s"(0x80000000u));
    return X;
}

constexpr int K1_W   = 98304;
constexpr int K1_LDS = 163840;
__host__ __device__ constexpr int K1_R(int r) { return r == 5 ? 0 : r * 32768; }

__device__ __forceinline__ void k1_gemm(char* smem, const unsigned lds0, const int lane, const int w, const unsigned g0,
                                        const unsigned xvo, const rsrc_t xr, const rsrc_t tr) {
    const int g = lane >> 4, n = lane & 15;
    const int fh = w & 1, rg = w >> 1;
    const unsigned ldw = lds0 + w * 1024;
    unsigned gr[6];
#pragma unroll
    for (int r = 0; r < 6; ++r) gr[r] = (g0 >> (4 * r)) & 15u;
#define K1_LDX(r, i) dma16(xr, xvo, gr[r] * 32768 + (i) * 8192, ldw + K1_R(r) + (i) * 8192)

    const f32x4 zero4 = {0.f, 0.f, 0.f, 0.f};
    bf16x8 wf[8][4];
    bf16x8 xq[8];
    f32x4 acc[2][4];
    const unsigned fr = ((n >> 3) & 1) | ((g & 1) << 1);
    const char* wp0 = smem + K1_W + (8 * g + (n >> 2)) * 128 + (((2 * fh) ^ fr) << 5) + 8 * (n & 3);
    const char* wp1 = smem + K1_W + (8 * g + (n >> 2)) * 128 + (((2 * fh + 1) ^ fr) << 5) + 8 * (n & 3);
    unsigned xa[3][4];
#pragma unroll
    for (int k = 0; k < 4; ++k) {
        xa[0][k] = lds0 + n * 512 + rg * 8192 + (((4 * k + g) ^ n) << 4);
        xa[1][k] = xa[0][k] + 65536; xa[2][k] = xa[0][k] + 131072;
    }
    const unsigned ovo = g * 1024 + fh * 128 + n * 8 + rg * 4096;
#define K1_RDW(ks) do { _Pragma("unroll") for (int mf = 0; mf < 4; ++mf) {                                   \
        const char* wp = ((mf & 1) ? wp1 : wp0) + (ks) * 8192 + (mf >> 1) * 4096;                           \
        u32x2 l2 = __builtin_bit_cast(u32x2, __builtin_amdgcn_ds_read_tr16_b64_v4i16((LDSP s16x4*)wp));      \
        u32x2 h2 = __builtin_bit_cast(u32x2, __builtin_amdgcn_ds_read_tr16_b64_v4i16((LDSP s16x4*)(wp + 512))); \
        wf[ks][mf] = __builtin_bit_cast(bf16x8, u32x4{l2.x, l2.y, h2.x, h2.y}); } } while (0)
#define K1_RDX(r, ks) xq[ks] = *(const LDSP bf16x8*)(size_t)(xa[K1_R(r) >> 16][(ks) & 3] + (K1_R(r) & 65535) + ((ks) >> 2) * 256)
#define K1_MM(p, ks) do { _Pragma("unroll") for (int m = 0; m < 4; ++m)                                      \
        acc[p][m] = __builtin_amdgcn_mfma_f32_16x16x32_bf16(xq[ks], wf[ks][m], (ks) ? acc[p][m] : zero4, 0, 0, 0); } while (0)
#define SB() __builtin_amdgcn_sched_barrier(0)

    WAIT_V(4); BAR();
#pragma unroll
    for (int ks = 0; ks < 6; ++ks) K1_RDW(ks);
#pragma unroll
    for (int i = 0; i < 4; ++i) K1_LDX(1, i);
    SB();
    WAIT_V(4); BAR();
#pragma unroll
    for (int ks = 0; ks < 8; ++ks) K1_RDX(0, ks);
    K1_RDW(6); K1_RDW(7);
    SB();
#pragma unroll
    for (int ks = 0; ks < 8; ++ks) {
        if (ks == 5) {
            WAIT_L0(); WAIT_V(4); BAR();
#pragma unroll
            for (int k = 0; k < 5; ++k) K1_RDX(1, k);
        }
        K1_MM(0, ks);
        if (ks >= 5) K1_RDX(1, ks);
        if (ks < 4) K1_LDX(2, ks);
        SB();
    }
#pragma unroll
    for (int r = 1; r < 6; ++r) {
        const int p = r & 1, q = p ^ 1;
        const unsigned vo = ovo + gr[r - 1] * 16384;
        u32x2 o;
#pragma unroll
        for (int ks = 0; ks < 8; ++ks) {
            if (r < 5 && ks == 4) {
                WAIT_V(r == 1 ? 6 : (r == 4 ? 4 : 8)); BAR();
#pragma unroll
                for (int k = 0; k < 4; ++k) K1_RDX(r + 1, k);
            }
            K1_MM(p, ks);
            if (r < 5 && ks >= 4) K1_RDX(r + 1, ks);
            o[ks & 1] = pack_t1(acc[q][ks & 1][ks >> 1], acc[q][2 + (ks & 1)][ks >> 1]);
            if (ks & 1) __builtin_amdgcn_raw_buffer_store_b64(o, tr, vo + (ks >> 1) * 256, 0, AUX_WT);
            if (r <= 3 && ks < 4) K1_LDX(r + 2, ks);
#pragma unroll
            for (int i = 0; i < 4; ++i) {
                __builtin_amdgcn_sched_group_barrier(0x008, 1, 0);
                __builtin_amdgcn_sched_group_barrier(0x002, 2, 0);
            }
            SB();
        }
    }
#pragma unroll
    for (int r = 0; r < 4; ++r) {
        const u32x2 o = {pack_t1(acc[1][0][r], acc[1][2][r]), pack_t1(acc[1][1][r], acc[1][3][r])};
        __builtin_amdgcn_raw_buffer_store_b64(o, tr, ovo + gr[5] * 16384 + r * 256, 0, AUX_WT);
    }
#undef K1_LDX
}

__global__ __launch_bounds__(512, 2)
void egc_k1(const uint16_t* __restrict__ x, const uint16_t* __restrict__ ea,
            const int* __restrict__ src, const int* __restrict__ dst,
            const uint16_t* __restrict__ uw, const uint16_t* __restrict__ vw,
            const uint16_t* __restrict__ gw,
            unsigned* __restrict__ T1, unsigned* __restrict__ T2,
            unsigned* __restrict__ recs, unsigned* __restrict__ tab,
            int N, int E, int nrb, int nbins, int ec, int nchunks, int rows, int bmul)
{
    extern __shared__ __attribute__((aligned(16))) char smem[];
    const int b   = blockIdx.x;
    const int slot = b >> 3;
    const int item = (b & 7) * nrb + slot;
    const int rb  = item >> 3;
    const int ct  = item & 7;
    const int tid = threadIdx.x;
    const int lane = tid & 63;
    const int w = __builtin_amdgcn_readfirstlane(tid >> 6);
    asm volatile(".Legc_code_begin:" :: "s"(x), "s"(ea), "s"(src), "s"(dst), "s"(uw), "s"(vw), "s"(gw), "s"(T1), "s"(T2), "s"(recs), "s"(tab),
                       "s"(N), "s"(E), "s"(nrb), "s"(nbins), "s"(ec), "s"(nchunks), "s"(rows), "s"(bmul));

    const int s = ct >> 1;
    const int row0 = rb * rows;
    const unsigned nr = (unsigned)max(min(rows, N - row0), 0);
    const unsigned lds0 = (unsigned)(size_t)(LDSP char*)smem;
    const rsrc_t xr = mk_rsrc(x + (size_t)row0 * D, nr * 512u);
    const unsigned rl = 2 * w + (lane >> 5);
    const unsigned xvo = rl * 512 + (((lane & 31) ^ rl) << 4);
    const unsigned g0 = (unsigned)(0x2154321543210ull >> (4 * ct)) & ~15u;
    {
        const uint16_t* wsel = (ct & 1) ? (w < 4 ? gw : uw) : (w < 4 ? gw + D * D : vw);
        const unsigned f = ((lane >> 4) & 1) | ((w & 1) << 1);
        const unsigned wvo = (lane >> 3) * 512 + s * 128 + ((((lane >> 1) & 3) ^ f) << 5) + (lane & 1) * 16 + (w & 3) * 4096;
        unsigned t0, t1;
#define K1_W1 "s_lshl_b32 %1, %0, 1\n\ts_add_u32 m0, %0, %[lw]\n\ts_add_u32 %0, %0, 0x2000\n\t"                  \
              "buffer_load_dwordx4 %[wvo], %[wr], %1 offen lds\n\ts_and_b32 %0, %0, 0xe000\n\t"
#define K1_X1(O) "s_add_u32 m0, %[lx], " #O "\n\ts_add_u32 %1, %[gx], " #O "\n\tbuffer_load_dwordx4 %[xvo], %[xr], %1 offen lds\n\t"
        asm volatile("s_cmp_ge_i32 %[slot], %[nrb]\n\t"
                     "s_cbranch_scc1 .Lk1skip_%=\n\t"
                     "s_mov_b32 %0, %[k0]\n\t"
                     K1_W1 K1_W1 K1_W1 K1_W1 K1_W1 K1_W1 K1_W1 K1_W1
                     "s_barrier\n\t"
                     K1_X1(0) K1_X1(0x2000) K1_X1(0x4000) K1_X1(0x6000)
                     ".Lk1skip_%=:"
                     : "=&s"(t0), "=&s"(t1)
                     : [slot] "s"(slot), [nrb] "s"(nrb), [k0] "s"((rb & 3) * 16384), [lw] "s"(lds0 + w * 1024 + K1_W),
                       [wvo] "v"(wvo), [wr] "s"(mk_rsrc(wsel, D * D * 2)), [lx] "s"(lds0 + w * 1024), [gx] "s"((g0 & 15u) * 32768),
                       [xvo] "v"(xvo), [xr] "s"(xr)
                     : "m0", "scc", "memory");
    }
    if (slot < nrb) {
        k1_gemm(smem, lds0, lane, w, g0, xvo, xr,
                mk_rsrc(((ct & 1) ? T2 : T1) + ((size_t)s * N + row0) * FS, nr * 256u));
        return;
    }
    {
        const int c = (slot - nrb) * 8 + (b & 7);
        {
            unsigned long long pc;
            unsigned len, back;
            asm volatile("s_getpc_b64 %0\n.Legc_code_here:\n\ts_mov_b32 %1, .Legc_code_end-.Legc_code_begin\n\t"
                         "s_mov_b32 %2, .Legc_code_here-.Legc_code_begin" : "=s"(pc), "=s"(len), "=s"(back));
            asm volatile("s_mov_b32 m0, %0\n\ts_nop 0\n\tbuffer_load_dword %1, %2, 0 offen lds"
                         :: "s"(lds0 + 131072 + w * 1024), "v"(tid * 64),
                            "s"(mk_rsrc((const void*)(pc - back), (back > 65536u || len > 65536u) ? 0u : len)) : "m0", "memory");
        }
        if (c == 0 && tid < 256) {
            const int g = lane >> 4, n = lane & 15;
            const uint16_t* cw = gw + 2 * D * D + w * FS + (n >> 3) * 32 + (n & 7) * 2 + (g >> 1) + (g & 1) * 16;
            const u32x2 cf = {pack_bf16(bf_lo(cw[0]) * NLOG2E, bf_lo(cw[D]) * NLOG2E),
                              pack_bf16(bf_lo(cw[2 * D]) * NLOG2E, bf_lo(cw[3 * D]) * NLOG2E)};
            __builtin_amdgcn_raw_buffer_store_b64(cf, mk_rsrc(tab + BINMAX * TSTR, 2048), tid * 8, 0, AUX_WT);
        }
        if (c >= nchunks) return;
        unsigned* cw  = (unsigned*)smem;
        unsigned* wt  = cw + 8 * BINMAX;
        unsigned* rme = wt + 16;
        u32x2*    rev = (u32x2*)(rme + RMAX * 2048);
        const int e0 = c * ec;
        const int eb = ((tid >> 4) * NCH + c) * 32 + (tid & 15) * 2;
        constexpr int RS = 32 * NCH * 32, NR = 2 * RMAX;
        const rsrc_t dr = mk_rsrc(dst, E * 4u), sr = mk_rsrc(src, E * 4u), er = mk_rsrc(ea, E * 8u);
        u32x2 dv[NR], sv[NR];
        u32x4 ev[NR];
#pragma unroll
        for (int r = 0; r < NR; ++r) dv[r] = __builtin_amdgcn_raw_buffer_load_b64(dr, eb * 4, r * RS * 4, 2);
        __builtin_amdgcn_sched_barrier(0);
#pragma unroll
        for (int r = 0; r < NR; ++r) {
            ev[r] = __builtin_amdgcn_raw_buffer_load_b128(er, eb * 8, r * RS * 8, 2);
            sv[r] = __builtin_amdgcn_raw_buffer_load_b64(sr, eb * 4, r * RS * 4, 2);
        }
        ((u32x4*)cw)[tid] = u32x4{0u, 0u, 0u, 0u};
        __builtin_amdgcn_sched_barrier(0);
        __syncthreads();
        unsigned* cmine = cw + w * BINMAX;
        unsigned rk[NR][2], bn[NR][2];
#pragma unroll
        for (int r = 0; r < NR; ++r)
#pragma unroll
            for (int k = 0; k < 2; ++k) {
                bn[r][k] = __umul24(dv[r][k], 53093u) >> 22;
                rk[r][k] = 0;
                if (r * RS + eb + k < E) rk[r][k] = atomicAdd(&cmine[bn[r][k]], 1u);
            }
        __syncthreads();
        {
            unsigned pre[8], t = 0, inc = 0;
            if (tid < BINMAX) {
#pragma unroll
                for (int k = 0; k < 8; ++k) { pre[k] = t; t += cw[k * BINMAX + tid]; }
                inc = wave_scan(t);
                if (lane == 63) wt[w] = inc;
            }
            __syncthreads();
            if (tid < BINMAX) {
                const u32x4 w4 = *(const u32x4*)wt;
                const unsigned st = inc - t + (w > 0 ? w4[0] : 0u) + (w > 1 ? w4[1] : 0u) + (w > 2 ? w4[2] : 0u);
#pragma unroll
                for (int k = 0; k < 8; ++k) cw[k * BINMAX + tid] = st + pre[k];
                if (tid < nbins) __builtin_amdgcn_raw_buffer_store_b32(st | (t << 16), mk_rsrc(tab, BINMAX * TSTR * 4), (tid * TSTR + c) * 4, 0, AUX_WT);
            }
        }
        __syncthreads();
#pragma unroll
        for (int r = 0; r < NR; ++r)
#pragma unroll
            for (int k = 0; k < 2; ++k) rk[r][k] += cmine[bn[r][k]];
#pragma unroll
        for (int r = 0; r < NR; ++r)
#pragma unroll
            for (int k = 0; k < 2; ++k)
                if (r * RS + eb + k < E) {
                    rev[rk[r][k]] = u32x2{ev[r][2 * k], ev[r][2 * k + 1]};
                    rme[rk[r][k]] = (sv[r][k] << 16) + (dv[r][k] - bn[r][k] * NBN + (__umul24(sv[r][k], bmul) >> 16) * NB) * 16u;
                }
        __syncthreads();
        const rsrc_t omr = mk_rsrc(recs + e0, ec * 4), oer = mk_rsrc(recs + NCH * ec + e0 * 2, ec * 8);
#pragma unroll
        for (int i = 0; i < RMAX * 3; ++i) {
            const u32x4 v = ((const u32x4*)rme)[tid + 512 * i];
            __builtin_amdgcn_raw_buffer_store_b128(v, i < RMAX ? omr : oer, tid * 16 + (i < RMAX ? i : i - RMAX) * 8192, 0, AUX_WT);
        }
        __builtin_amdgcn_s_waitcnt(0);
    }
}

constexpr int K2_OFFS = 0;
constexpr int K2_EV   = K2_OFFS + 32 * LSTR * 4;
constexpr int K2_CNT  = K2_EV + 33 * LSTR * 8;
constexpr int K2_HIST = K2_CNT + NBK * NB * 16;
constexpr int K2_ORD  = K2_HIST + 256;
constexpr int K2_LEN  = K2_ORD + 128;
constexpr int K2_FLAG = K2_LEN + 32;
constexpr int K2_PF   = K2_FLAG + 16;
constexpr int K2_LDS  = K2_PF + 8192;
static_assert((K2_PF - K2_EV) % 16 == 0 && K2_EV % 16 == 0, "LDS map");

__device__ __forceinline__ u32x2 silu_pack(f32x4 y) {
    f32x4 o;
#pragma unroll
    for (int r = 0; r < 4; ++r) {
        float t = __builtin_amdgcn_exp2f(y[r] * NLOG2E);
        o[r] = y[r] * __builtin_amdgcn_rcpf(1.0f + t);
    }
    return u32x2{pack_bf16(o[0], o[1]), pack_bf16(o[2], o[3])};
}

__device__ __forceinline__ void k2_slow(char* smem, const unsigned* __restrict__ T1, const unsigned* __restrict__ T2,
                                        const uint16_t* __restrict__ gw, const uint16_t* __restrict__ ea,
                                        const int* __restrict__ src, const int* __restrict__ dst,
                                        uint16_t* __restrict__ out, int N, int E, int s, int node0) {
    float* accs = (float*)smem;
    unsigned* queue = (unsigned*)(smem + NB * FS * 4);
    static_assert(NB * FS * 4 + 2048 <= K2_CNT, "slow-path LDS");
    const unsigned* t2g = T2 + ((size_t)s * N + node0) * FS;
    const int tid = threadIdx.x, f = tid & 63, w = tid >> 6;
    __syncthreads();
    for (int i = tid; i < NB * FS; i += 512) accs[i] = 0.f;
    const uint16_t* cf = gw + (size_t)2 * D * D + s * FS + (f & 32) + (f & 1) * 16 + ((f & 31) >> 1);
    const float c0 = bf_lo(cf[0]) * NLOG2E, c1 = bf_lo(cf[D]) * NLOG2E;
    const float c2 = bf_lo(cf[2 * D]) * NLOG2E, c3 = bf_lo(cf[3 * D]) * NLOG2E;
#pragma unroll 1
    for (int eb = 0; eb < E; eb += 512) {
        __syncthreads();
        const int e = eb + tid;
        const int dn = (e < E) ? dst[e] - node0 : -1;
        queue[tid] = (dn >= 0 && dn < NBN) ? (unsigned)dn : 0xffffffffu;
        __syncthreads();
#pragma unroll 1
        for (int q0 = 0; q0 < 512; q0 += 64) {
            const unsigned qv = queue[q0 + f];
            unsigned long long m = __ballot(qv != 0xffffffffu && (qv & 7u) == (unsigned)w);
            while (m) {
                const int l = __builtin_ctzll(m);
                m &= m - 1;
                const int ee = eb + q0 + l;
                const int d2 = __builtin_amdgcn_readlane(qv, l);
                const unsigned tv = T1[((size_t)s * N + src[ee]) * FS + f];
                const u32x2 e2 = *(const u32x2*)(ea + (size_t)ee * 4);
                const float z = bf_lo(t2g[d2 * FS + f]) + bf_lo(tv) + bf_lo(e2.x) * c0 + bf_hi(e2.x) * c1
                                + bf_lo(e2.y) * c2 + bf_hi(e2.y) * c3;
                accs[d2 * FS + f] += __builtin_bit_cast(float, tv) * __builtin_amdgcn_rcpf(1.0f + __builtin_amdgcn_exp2f(z));
            }
        }
    }
    __syncthreads();
#pragma unroll 1
    for (int p = tid; p < NBN * 16; p += 512) {
        const int r = p >> 4, q4 = (p & 15) * 4;
        if (node0 + r < N) {
            f32x4 y;
#pragma unroll
            for (int k = 0; k < 4; ++k) {
                const int sl = r * FS + (q4 & 32) + ((q4 & 15) + k) * 2 + ((q4 >> 4) & 1);
                y[k] = accs[sl] + __builtin_bit_cast(float, t2g[sl]);
            }
            *(u32x2*)(out + (size_t)(node0 + r) * D + s * FS + q4) = silu_pack(y);
        }
    }
}

__global__ __launch_bounds__(512, 4)
void egc_k2(const unsigned* __restrict__ T1, const unsigned* __restrict__ T2,
            const unsigned* __restrict__ recs, const unsigned* __restrict__ tab,
            const uint16_t* __restrict__ gw, const uint16_t* __restrict__ ea,
            const int* __restrict__ src, const int* __restrict__ dst,
            uint16_t* __restrict__ out, int N, int E, int nchunks, int nbins, int ec, int force_slow)
{
    __shared__ __attribute__((aligned(16))) char smem[K2_LDS];
    unsigned* hist = (unsigned*)(smem + K2_HIST);
    unsigned* flag = (unsigned*)(smem + K2_FLAG);

    asm volatile("" :: "s"(T1), "s"(T2), "s"(recs), "s"(tab), "s"(out), "s"(N), "s"(nchunks),
                       "s"(nbins), "s"(ec), "s"(force_slow));
    const int b   = blockIdx.x;
    const int s   = (b & 7) >> 1;
    const int hb  = (nbins + 1) >> 1;
    const int bin = (b & 1) * hb + (b >> 3);
    const int tid = threadIdx.x;
    const int lane = tid & 63;
    const int w = __builtin_amdgcn_readfirstlane(tid >> 6);
    const int g = lane >> 4, n = lane & 15;
    const int node0 = bin * NBN;
    const unsigned lds0 = (unsigned)(size_t)(LDSP char*)smem;
    const rsrc_t t1r = mk_rsrc(T1 + (size_t)s * N * FS, (unsigned)N * 256u);
    __builtin_amdgcn_s_setprio(3);

    const unsigned c = __umul24(tid, 5462u) >> 16, j = tid - c * TPC;
    unsigned te;
    asm volatile("buffer_load_dword %0, %1, %2, 0 offen"
                 : "=&v"(te) : "v"(c * 4), "s"(mk_rsrc(tab + bin * TSTR, bin < nbins ? nchunks * 4 : 0)) : "memory");
    dma16(t1r, (b >> 3) * 8192u + tid * 16u, 0, lds0 + K2_PF + w * 1024);
    {
        const u32x4 sv = {SENT, SENT, SENT, SENT}, zv = {0u, 0u, 0u, 0u};
        u32x4* po = (u32x4*)(smem + K2_OFFS);
        constexpr int NO = 32 * LSTR / 4, NZ = (K2_PF - K2_EV) / 16;
        po[tid] = sv;
        if (tid < NO - 512) po[tid + 512] = sv;
        u32x4* pz = (u32x4*)(smem + K2_EV);
#pragma unroll
        for (int i = 0; i < NZ / 512; ++i) pz[tid + 512 * i] = zv;
        if (tid < NZ % 512) pz[tid + NZ / 512 * 512] = zv;
    }
    u32x4 asel;
#pragma unroll
    for (int k = 0; k < 4; ++k) asel[k] = (n == 4 * g + k) ? 0x3f80u : 0u;
    u32x2 lo16 = {(unsigned)n * 16u, (unsigned)n * 16u};
    unsigned vop = lds0 + K2_OFFS + (w * 4 + g) * (LSTR * 4);
    unsigned vep = lds0 + K2_EV + (((n & 3) == g) ? w * 4 + (n >> 2) : 32) * (LSTR * 8);
    unsigned olane = (n >> 3) * 64 + (n & 6) * 4 + (n & 1) * 32;
    unsigned j4 = 4 * j, rb0 = __umul24(c, ec) + j4;
    const rsrc_t mr = mk_rsrc(recs, NCH * 4u * ec), er = mk_rsrc(recs + NCH * ec, NCH * 8u * ec);
    asm volatile("s_waitcnt vmcnt(1)" : "+v"(te), "+v"(asel), "+v"(lo16), "+v"(vop), "+v"(vep), "+v"(olane), "+v"(j4), "+v"(rb0)
                 : "s"(mr), "s"(er) : "memory");
    const int nc = (int)(te >> 16) - (int)j4;
    u32x4 rm[2], re[4];
    const unsigned r0 = nc > 0 ? (rb0 + (te & 0xffffu)) * 4u : OOB / 2;
    asm volatile("buffer_load_dwordx4 %0, %1, %2, 0 offen" : "=&v"(rm[0]) : "v"(r0), "s"(mr) : "memory");
    __builtin_amdgcn_sched_barrier(0);
    unsigned long long vm[SLOTS];
#pragma unroll
    for (int i = 0; i < SLOTS; ++i) vm[i] = __builtin_amdgcn_ballot_w64(nc > (i >> 2) * (4 * TPC) + (i & 3));
    const bool big = vm[4] != 0;
    asm volatile("s_cmp_eq_u64 %3, 0\n\t"
                 "s_cbranch_scc1 .Lnb_%=\n\t"
                 "buffer_load_dwordx4 %0, %1, %2, 0 offen offset:192\n"
                 ".Lnb_%=:"
                 : "=&v"(rm[1]) : "v"(r0), "s"(mr), "s"(vm[4]) : "scc", "memory");
    if (bin >= nbins) { WAIT_V(0); return; }
    __syncthreads();
    if (nc > TPC * SLOTS || force_slow) flag[0] = 1u;
    asm volatile("s_waitcnt vmcnt(0)" : "+v"(rm[0]), "+v"(rm[1]) :: "memory");
    u32x2 cfv;
    asm volatile("s_cmp_eq_u64 %4, 0\n\t"
                 "s_cbranch_scc1 .Lnb_%=\n\t"
                 "buffer_load_dwordx4 %0, %2, %3, 0 offen offset:384\n\t"
                 "buffer_load_dwordx4 %1, %2, %3, 0 offen offset:400\n"
                 ".Lnb_%=:"
                 : "=&v"(re[2]), "=&v"(re[3]) : "v"(r0 * 2), "s"(er), "s"(vm[4]) : "scc", "memory");
    asm volatile("buffer_load_dwordx4 %0, %3, %5, 0 offen\n\t"
                 "buffer_load_dwordx4 %1, %3, %5, 0 offen offset:16\n\t"
                 "buffer_load_dwordx2 %2, %4, %6, 0 offen"
                 : "=&{v[108:111]}"(re[0]), "=&{v[112:115]}"(re[1]), "=&{v[14:15]}"(cfv)
                 : "v"(r0 * 2), "v"(lane * 8), "s"(er), "s"(mk_rsrc(tab + BINMAX * TSTR + s * 128, 512)) : "memory");
    unsigned ca[SLOTS], rk[SLOTS];
#define K2_CA(I) (lds0 + K2_CNT + (w & 4) * 2 + (rm[(I) >> 2][(I) & 3] & 0xffffu))
#pragma unroll
    for (int i = 0; i < 4; ++i) ca[i] = K2_CA(i);
#define K2_AT(R, A, M) "s_mov_b64 exec, %[" #M "]\n\tds_add_rtn_u32 %[" #R "], %[" #A "], %[one]\n\t"
#define K2_AT4(O) asm volatile(K2_AT(r0, a0, m0) K2_AT(r1, a1, m1) K2_AT(r2, a2, m2) K2_AT(r3, a3, m3)                   \
                 "s_mov_b64 exec, -1\n\ts_waitcnt lgkmcnt(0)"                                                            \
                 : [r0] "=&v"(rk[O]), [r1] "=&v"(rk[O + 1]), [r2] "=&v"(rk[O + 2]), [r3] "=&v"(rk[O + 3])                \
                 : [a0] "v"(ca[O]), [a1] "v"(ca[O + 1]), [a2] "v"(ca[O + 2]), [a3] "v"(ca[O + 3]),                       \
                   [m0] "s"(vm[O]), [m1] "s"(vm[O + 1]), [m2] "s"(vm[O + 2]), [m3] "s"(vm[O + 3]),                       \
                   [one] "v"(1u << (8 * (w & 3))) : "memory")
    K2_AT4(0);
    asm volatile("" : "=v"(rk[4]), "=v"(rk[5]), "=v"(rk[6]), "=v"(rk[7]));
    if (big) {
#pragma unroll
        for (int i = 4; i < SLOTS; ++i) ca[i] = K2_CA(i);
        K2_AT4(4);
    }
    __syncthreads();
    if (w >= 4) {
#pragma unroll
        for (int i = 0; i < 2; ++i)
            dma16(t1r, (b >> 3) * 4096u + (tid - 256) * 16u, hb * 4096u * (i + 2), lds0 + K2_PF + w * 1024);
    }

    unsigned pre[2 * NBK], deg = 0, key = 0, rank = 0;
    if (tid < NB) {
        u32x4 f4[NBK];
#pragma unroll
        for (int k = 0; k < NBK; ++k) f4[k] = *(const u32x4*)(smem + K2_CNT + (k * NB + tid) * 16);
#pragma unroll
        for (int k = 0; k < NBK; ++k) {
            pre[2 * k] = deg;
            pre[2 * k + 1] = __builtin_amdgcn_sad_u8(f4[k][0], 0u, deg);
            deg = __builtin_amdgcn_sad_u8(f4[k][2], 0u, pre[2 * k + 1]);
        }
        key = 63u - min(deg, 63u);
        rank = (atomicAdd(&hist[key], 1u << (16 * w)) >> (16 * w)) & 0xffffu;
    }
    {
        unsigned long long bad = 0;
#pragma unroll
        for (int i = 0; i < 4; ++i) {
            rk[i] = (rk[i] >> (8 * (w & 3))) & 255u;
            bad |= __builtin_amdgcn_ballot_w64(rk[i] > 63u) & vm[i];
        }
        if (big) {
#pragma unroll
            for (int i = 4; i < SLOTS; ++i) {
                rk[i] = (rk[i] >> (8 * (w & 3))) & 255u;
                bad |= __builtin_amdgcn_ballot_w64(rk[i] > 63u) & vm[i];
            }
        }
        if (bad) flag[0] = 1u;
    }
    __syncthreads();
    unsigned wv = 0, pos = 0;
    if (w < 2) {
        const unsigned hv = hist[lane], h1 = hv >> 16, ht = (hv & 0xffffu) + h1;
        const unsigned ex = wave_scan(ht) - (w ? h1 : ht);
        rank += __builtin_amdgcn_ds_bpermute(key * 4, ex);
        pos = rank >> 4;
        wv = ((rank >> 2) & 3u) ^ (pos ? 3u : 0u);
        if (pos >= 2) { wv += 4; pos -= 2; }
        if (tid < NB) {
            smem[K2_ORD + wv * 16 + (rank & 3u) * 4 + pos] = (char)tid;
            if ((rank & 3u) == 0) smem[K2_LEN + wv * 4 + pos] = (char)min(max((deg + 1u) & ~1u, MINL), 254u);
        }
    }
    __syncthreads();
    if (tid < NB) {
        const unsigned l4 = *(const unsigned*)(smem + K2_LEN + wv * 4);
        const unsigned first = __builtin_amdgcn_sad_u8(l4 & ((1u << (8 * pos)) - 1u), 0u, 0u);
        const unsigned base = (wv * 4 + (rank & 3u)) * LSTR + first;
        if (first + max(deg, MINL) > MAXT) flag[0] = 1u;
#pragma unroll
        for (int k = 0; k < NBK; ++k) {
            *(unsigned*)(smem + K2_CNT + (k * NB + tid) * 16 + 4) = base + pre[2 * k];
            *(unsigned*)(smem + K2_CNT + (k * NB + tid) * 16 + 12) = base + pre[2 * k + 1];
        }
    }
    unsigned lens = __builtin_amdgcn_readfirstlane(*(const unsigned*)(smem + K2_LEN + w * 4));
    unsigned ordb = *(const unsigned*)(smem + K2_ORD + w * 16 + g * 4);
    const rsrc_t t2r = mk_rsrc(T2 + ((size_t)s * N + node0) * FS, (unsigned)(N - node0) * 256u);
    dma16(t2r, (ordb & 255u) * 256u + lo16[0], 0, lds0 + K2_PF + w * 1024);
    __syncthreads();

    u32x2 cs[SLOTS];
#pragma unroll
    for (int i = 0; i < 4; ++i) cs[i] = *(const LDSP u32x2*)(size_t)ca[i];
    const unsigned slow = __builtin_amdgcn_readfirstlane(flag[0]);
#pragma unroll
    for (int i = 0; i < SLOTS; ++i) vm[i] = slow ? 0ull : vm[i];
    unsigned p8[4];
    {
        const unsigned lowm = (1u << (8 * (w & 3))) - 1u;
#pragma unroll
        for (int i = 0; i < 4; ++i) {
            const unsigned p = __builtin_amdgcn_sad_u8(cs[i][0] & lowm, 0u, rk[i]) + cs[i][1];
            asm volatile("s_mov_b64 exec, %2\n\tds_write_b32 %0, %1 offset:%c3\n\ts_mov_b64 exec, -1"
                         :: "v"(p * 4), "v"((rm[0][i] >> 16) << 8), "s"(vm[i]), "i"(K2_OFFS) : "memory");
            p8[i] = p * 8;
        }
        if (big) {
            asm volatile("s_waitcnt vmcnt(3)" : "+v"(re[2]), "+v"(re[3]) :: "memory");
#pragma unroll
            for (int i = 4; i < SLOTS; ++i) cs[i] = *(const LDSP u32x2*)(size_t)K2_CA(i);
#pragma unroll
            for (int i = 4; i < SLOTS; ++i) {
                const unsigned p = __builtin_amdgcn_sad_u8(cs[i][0] & lowm, 0u, rk[i]) + cs[i][1];
                const u32x2 e2 = {re[i >> 1][(i & 1) * 2], re[i >> 1][(i & 1) * 2 + 1]};
                asm volatile("s_mov_b64 exec, %4\n\tds_write_b32 %0, %1 offset:%c5\n\tds_write_b64 %2, %3 offset:%c6\n\ts_mov_b64 exec, -1"
                             :: "v"(p * 4), "v"((rm[1][i & 3] >> 16) << 8), "v"(p * 8), "v"(e2), "s"(vm[i]), "i"(K2_OFFS), "i"(K2_EV) : "memory");
            }
        }
    }
    const rsrc_t outr = mk_rsrc(out + (size_t)node0 * D + s * FS, (unsigned)min(N - node0, NBN) * 512u - s * 128);
    WAIT_L0();
    __syncthreads();
    unsigned tot = (lens & 255) + ((lens >> 8) & 255) + ((lens >> 16) & 255);
    lens >>= 1;

#define V2(a, b) "v[" #a ":" #b "]"
#define K2_EXP2(a, b) "v_exp_f32 v" #a ", v" #a "\n\tv_exp_f32 v" #b ", v" #b "\n\t"
#define K2_RCP4(a, b, c, d) "v_rcp_f32 v" #a ", v" #a "\n\tv_rcp_f32 v" #b ", v" #b "\n\tv_rcp_f32 v" #c ", v" #c "\n\tv_rcp_f32 v" #d ", v" #d "\n\t"
#define K2_ADD1(x) "v_pk_add_f32 " x ", " x ", 1.0 op_sel_hi:[1,0]\n\t"
#define K2_FMA(d, x, y, z) "v_pk_fma_f32 " d ", " x ", " y ", " z "\n\t"
#define K2_RF(TL, TH, HL, HH)                                                     \
        K2_FMA("v[22:23]", "v[22:23]", TL, "v[22:23]") K2_FMA("v[24:25]", "v[24:25]", TH, "v[24:25]")         \
        K2_FMA("v[22:23]", HL, "v[104:105]", "v[22:23]") K2_FMA("v[24:25]", HH, "v[106:107]", "v[24:25]")     \
        K2_FMA("v[104:105]", "v[104:105]", TL, "v[104:105]") K2_FMA("v[106:107]", "v[106:107]", TH, "v[106:107]")
#define K2_FAST(A0, A1, A2, A3, B0, B1, B2, B3, HAL, HAH, HBL, HBH, ADD)          \
        K2_RF(V2(B0, B1), V2(B2, B3), HBL, HBH) ADD K2_RF(V2(A0, A1), V2(A2, A3), HAL, HAH)
#define K2_SAFE(A0, A1, A2, A3, B0, B1, B2, B3, HAL, HAH, HBL, HBH, ADD)          \
        K2_ADD1(V2(B0, B1)) K2_ADD1(V2(B2, B3)) K2_ADD1(V2(A0, A1)) K2_ADD1(V2(A2, A3)) \
        ADD                                                                       \
        K2_RCP4(A0, A1, A2, A3) K2_RCP4(B0, B1, B2, B3)                           \
        K2_FMA("v[22:23]", V2(A0, A1), HAL, "v[22:23]") K2_FMA("v[24:25]", V2(A2, A3), HAH, "v[24:25]") \
        K2_FMA("v[22:23]", V2(B0, B1), HBL, "v[22:23]") K2_FMA("v[24:25]", V2(B2, B3), HBH, "v[24:25]")
#define K2_RAL "v[38:39]"
#define K2_RAH "v[40:41]"
#define K2_RBL "v[42:43]"
#define K2_RBH "v[44:45]"
#define K2_YES(x) x
#define K2_NO(x) ""
#define K2_PAIR(LB, MATH, LD, VW, A0, A1, A2, A3, B0, B1, B2, B3, NA, NB_, H0, H1, H2, H3, H4, H5, H6, H7, DNA, DNB, EK, ERD, AL, AH, ADD, ARD) \
        "s_sub_u32 s66, s66, 1\n\t"                                               \
        "s_cbranch_scc1 .Lsw" #LB "_%=\n"                                          \
        ".Lrt" #LB "_%=:\n\t"                                                      \
        LD("s_waitcnt lgkmcnt(2)\n\t")                                            \
        "v_mfma_f32_16x16x16_bf16 " NA ", " EK##L ", v[14:15], v[18:21]\n\t"      \
        K2_EXP2(B0, B1)                                                           \
        "v_mfma_f32_16x16x16_bf16 " NB_ ", " EK##H ", v[14:15], v[18:21]\n\t"     \
        ERD                                                                       \
        K2_EXP2(B2, B3)                                                           \
        "s_waitcnt " VW "\n\t"                                                    \
        "v_mfma_f32_16x16x32_bf16 " NA ", v[8:11], " DNA ", " NA "\n\t"           \
        K2_EXP2(A0, A1)                                                           \
        "v_mfma_f32_16x16x32_bf16 " NB_ ", v[8:11], " DNB ", " NB_ "\n\t"         \
        K2_EXP2(A2, A3)                                                           \
        MATH(A0, A1, A2, A3, B0, B1, B2, B3, V2(H0, H1), V2(H2, H3), V2(H4, H5), V2(H6, H7), LD(ADD)) \
        LD("buffer_load_dwordx4 " V2(H0, H3) ", " AL ", %[t1r], 0 offen\n\t")     \
        LD("buffer_load_dwordx4 " V2(H4, H7) ", " AH ", %[t1r], 0 offen\n\t")     \
        LD(ARD)
#define K2_BODY(L, MATH, LD, W0, W1, W2, W3)                                      \
        K2_PAIR(L##0, MATH, LD, W0, 30, 31, 32, 33, 34, 35, 36, 37, "v[96:99]", "v[100:103]", 64, 65, 66, 67, 68, 69, 70, 71,   \
                "v[72:75]", "v[76:79]", K2_RB, K2_EB(48), "v46", "v47", K2_ADDA, K2_AA(48))                                \
        K2_PAIR(L##1, MATH, LD, W1, 96, 97, 98, 99, 100, 101, 102, 103, "v[30:33]", "v[34:37]", 72, 73, 74, 75, 76, 77, 78, 79, \
                "v[80:83]", "v[84:87]", K2_RA, LD(K2_EA(64)), "v48", "v49", K2_ADDB, K2_AB(56))                            \
        K2_PAIR(L##2, MATH, LD, W2, 30, 31, 32, 33, 34, 35, 36, 37, "v[96:99]", "v[100:103]", 80, 81, 82, 83, 84, 85, 86, 87,   \
                "v[88:91]", "v[92:95]", K2_RB, LD(K2_EB(80)), "v46", "v47", K2_ADDA, K2_AA(64))                            \
        K2_PAIR(L##3, MATH, LD, W3, 96, 97, 98, 99, 100, 101, 102, 103, "v[30:33]", "v[34:37]", 88, 89, 90, 91, 92, 93, 94, 95, \
                "v[64:67]", "v[68:71]", K2_RA, LD(K2_EA(96)), "v48", "v49", K2_ADDB, K2_AB(72))
#define K2_LOOP(L, MATH)                                                          \
        ".Lloop" #L "_%=:\n\t"                                                    \
        K2_BODY(L, MATH, K2_YES, "vmcnt(4)", "vmcnt(4)", "vmcnt(4)", "vmcnt(4)")  \
        "v_add_u32 v53, 64, v53\n\t"                                              \
        "v_add_u32 v52, 32, v52\n\t"                                              \
        "s_cmp_eq_u32 s71, 0\n\t"                                                 \
        "s_cbranch_scc1 .Lt2d" #L "_%=\n\t"                                       \
        "s_mov_b32 s71, 0\n\t"                                                    \
        K2_NEXT_T2                                                                \
        ".Lt2d" #L "_%=:\n\t"                                                     \
          \
        "s_sub_u32 s65, s65, 8\n\t"                                               \
        "s_cmp_gt_i32 s65, 20\n\t"                                            \
        "s_cbranch_scc1 .Lloop" #L "_%=\n\t"                                      \
        "s_setprio 1\n\t"                                                         \
        "s_cmp_gt_i32 s65, 8\n\t"                                                 \
        "s_cbranch_scc1 .Lloop" #L "_%=\n\t"                                      \
        "s_setprio 0\n\t"                                                         \
          \
        "s_waitcnt lgkmcnt(0)\n\t"                                                \
        K2_BODY(L##t, MATH, K2_NO, "vmcnt(4)", "vmcnt(2) lgkmcnt(0)", "vmcnt(0)", "vmcnt(0)") \
        "s_branch .Lflush_%=\n"                                                   \
        K2_SW(L##0) K2_SW(L##1) K2_SW(L##2) K2_SW(L##3) K2_SW(L##t0) K2_SW(L##t1) K2_SW(L##t2) K2_SW(L##t3)
#define K2_SW(K) ".Lsw" #K "_%=:\n\ts_swappc_b64 s[62:63], s[60:61]\n\ts_branch .Lrt" #K "_%=\n"
#define K2_ADDA "v_pk_add_f32 v[46:47], v[46:47], v[50:51]\n\t"
#define K2_ADDB "v_pk_add_f32 v[48:49], v[48:49], v[50:51]\n\t"
#define K2_EA(O) "ds_read_b128 v[38:41], v53 offset:" #O "\n\t"
#define K2_EB(O) "ds_read_b128 v[42:45], v53 offset:" #O "\n\t"
#define K2_AA(O) "ds_read_b64 v[46:47], v52 offset:" #O "\n\t"
#define K2_AB(O) "ds_read_b64 v[48:49], v52 offset:" #O "\n\t"
#define K2_NEXT_T2                                                                \
        "v_and_b32 v57, 0xff, v54\n\t"                                            \
        "v_lshrrev_b32 v54, 8, v54\n\t"                                           \
        "v_lshl_add_u32 v58, v57, 8, v50\n\t"                                     \
        "v_lshl_add_u32 v59, v57, 9, v7\n\t"                                      \
        "buffer_load_dwordx4 v[26:29], v58, %[t2r], 0 offen\n\t"
#define K2_CIN  "v_lshlrev_b32 v18, 16, v26\n\tv_lshlrev_b32 v19, 16, v27\n\tv_lshlrev_b32 v20, 16, v28\n\tv_lshlrev_b32 v21, 16, v29\n\t"
#define K2_ACC  "v_mov_b64 v[22:23], v[26:27]\n\tv_mov_b64 v[24:25], v[28:29]\n\tv_mov_b32 v56, v59\n\t"    \
                "v_mov_b64 v[104:105], s[72:73]\n\tv_mov_b64 v[106:107], s[72:73]\n\t"
#define K2_LD(D0, D3, A) "buffer_load_dwordx4 v[" #D0 ":" #D3 "], v" #A ", %[t1r], 0 offen\n\t"
    asm volatile(
        "s_cmp_lg_u32 %[slow], 0\n\t"
        "s_cbranch_scc1 .Ldone_%=\n\t"
        "s_mov_b64 s[74:75], 0\n\t"
        "s_mov_b32 s70, 0\n"
        ".Lstart_%=:\n\t"
        "s_waitcnt lgkmcnt(0)\n\t"
        "ds_read_b128 v[30:33], %[vop]\n\t"
        "ds_read_b128 v[34:37], %[vop] offset:16\n\t"
        "v_mov_b32 v54, %[ordb]\n\t"
        "v_mov_b32 v53, %[vep]\n\t"
        "v_mov_b32 v52, %[vop]\n\t"
        "s_mov_b32 s64, %[lens]\n\t"
        "s_mov_b32 s65, %[tot]\n\t"
        K2_NEXT_T2
        "s_getpc_b64 s[60:61]\n"
        ".Lpc_%=:\n\t"
        "s_add_u32 s60, s60, .Lhnd_%=-.Lpc_%=\n\t"
        "s_addc_u32 s61, s61, 0\n\t"
        "s_and_b32 s66, s64, 0xff\n\t"
        "s_sub_u32 s66, s66, 1\n\t"
        "s_mov_b32 s67, 0\n\t"
        "s_mov_b32 s68, 0xbfb8aa3b\n\t"
        "s_mov_b32 s72, 1.0\n\t"
        "s_mov_b32 s73, 1.0\n\t"
        "s_mov_b32 s69, 0x7b800000\n\t"
        "s_waitcnt lgkmcnt(0)\n\t"
        "v_pk_add_f32 v[30:31], v[30:31], v[50:51]\n\t"
        "v_pk_add_f32 v[32:33], v[32:33], v[50:51]\n\t"
        "v_pk_add_f32 v[34:35], v[34:35], v[50:51]\n\t"
        "v_pk_add_f32 v[36:37], v[36:37], v[50:51]\n\t"
        K2_LD(64, 67, 30) K2_LD(68, 71, 31) K2_LD(72, 75, 32) K2_LD(76, 79, 33)
        "s_cmp_lg_u32 s70, 0\n\t"
        "s_cbranch_scc1 .Lnoe_%=\n\t"
        "s_waitcnt vmcnt(5)\n\t"
        "s_mov_b64 exec, %[m0]\n\tds_write_b64 %[q0], v[108:109] offset:%c[evo]\n\t"
        "s_mov_b64 exec, %[m1]\n\tds_write_b64 %[q1], v[110:111] offset:%c[evo]\n\t"
        "s_mov_b64 exec, %[m2]\n\tds_write_b64 %[q2], v[112:113] offset:%c[evo]\n\t"
        "s_mov_b64 exec, %[m3]\n\tds_write_b64 %[q3], v[114:115] offset:%c[evo]\n\t"
        "s_mov_b64 exec, -1\n\t"
        "s_waitcnt lgkmcnt(0)\n\t"
        "s_barrier\n"
        ".Lnoe_%=:\n\t"
        K2_EA(0) K2_EB(16) K2_AA(32) K2_AB(40)
        "s_setprio 2\n\t"
        "s_waitcnt vmcnt(4) lgkmcnt(3)\n\t"
        K2_CIN K2_ACC
        "v_mfma_f32_16x16x16_bf16 v[96:99], v[38:39], v[14:15], v[18:21]\n\t"
        "v_mfma_f32_16x16x16_bf16 v[100:103], v[40:41], v[14:15], v[18:21]\n\t"
        K2_EA(32)
        "s_waitcnt vmcnt(2)\n\t"
        K2_LD(80, 83, 34) K2_LD(84, 87, 35) K2_LD(88, 91, 36) K2_LD(92, 95, 37)
        "v_mfma_f32_16x16x32_bf16 v[30:33], v[8:11], v[64:67], v[96:99]\n\t"
        "v_mfma_f32_16x16x32_bf16 v[34:37], v[8:11], v[68:71], v[100:103]\n\t"
        "s_mov_b32 s71, 1\n\t"
        "s_cmp_ge_u32 s66, 7\n\t"
        "s_cbranch_scc1 .Lt2l_%=\n\t"
        "s_mov_b32 s71, 0\n\t"
        K2_NEXT_T2
        ".Lt2l_%=:\n\t"
        "s_cmp_lg_u64 s[74:75], 0\n\t"
        "s_nop 2\n\t"
        "s_cbranch_scc1 .Lloopsf_%=\n"
        K2_LOOP(fa, K2_FAST)
        ".Lhnd_%=:\n\t"
        "s_cmp_lg_u32 s67, 0\n\t"
        "s_cbranch_scc1 .Lflush_%=\n\t"
        K2_CIN
        "s_mov_b32 s66, 0\n\t"
        "s_mov_b32 s67, 1\n\t"
        "s_setpc_b64 s[62:63]\n"
        ".Lflush_%=:\n\t"
        "v_pk_fma_f32 v[60:61], v[22:23], 0, v[104:105] op_sel_hi:[1,0,1]\n\t"
        "v_pk_fma_f32 v[62:63], v[24:25], 0, v[106:107] op_sel_hi:[1,0,1]\n\t"
        K2_RCP4(104, 105, 106, 107)
        "v_pk_add_f32 v[60:61], v[60:61], v[62:63]\n\t"
        "v_pk_mul_f32 v[22:23], v[22:23], v[104:105]\n\t"
        "v_add_f32 v60, v60, v61\n\t"
        "v_pk_mul_f32 v[24:25], v[24:25], v[106:107]\n\t"
        "v_cmp_nge_f32 vcc, s69, v60\n\t"
        "s_andn2_b64 vcc, vcc, s[74:75]\n\t"
        "s_cbranch_scc1 .Lredo_%=\n\t"
        "v_pk_mul_f32 v[60:61], v[22:23], s[68:69] op_sel_hi:[1,0]\n\t"
        "v_pk_mul_f32 v[62:63], v[24:25], s[68:69] op_sel_hi:[1,0]\n\t"
        "v_exp_f32 v60, v60\n\t"
        "v_exp_f32 v61, v61\n\t"
        "v_exp_f32 v62, v62\n\t"
        "v_exp_f32 v63, v63\n\t"
        "v_pk_add_f32 v[60:61], v[60:61], 1.0 op_sel_hi:[1,0]\n\t"
        "v_pk_add_f32 v[62:63], v[62:63], 1.0 op_sel_hi:[1,0]\n\t"
        "v_rcp_f32 v60, v60\n\t"
        "v_rcp_f32 v61, v61\n\t"
        "v_rcp_f32 v62, v62\n\t"
        "v_rcp_f32 v63, v63\n\t"
        "v_pk_mul_f32 v[60:61], v[60:61], v[22:23]\n\t"
        "v_pk_mul_f32 v[62:63], v[62:63], v[24:25]\n\t"
        "v_cvt_pk_bf16_f32 v62, v60, v62\n\t"
        "v_cvt_pk_bf16_f32 v63, v61, v63\n\t"
        "s_mov_b32 vcc_lo, 0x55555555\n\t"
        "s_mov_b32 vcc_hi, 0x55555555\n\t"
        "v_cndmask_b32_dpp v60, v63, v62, vcc quad_perm:[1,0,3,2] row_mask:0xf bank_mask:0xf\n\t"
        "s_not_b64 vcc, vcc\n\t"
        "v_cndmask_b32_dpp v61, v62, v63, vcc quad_perm:[1,0,3,2] row_mask:0xf bank_mask:0xf\n\t"
        "s_lshr_b32 s64, s64, 8\n\t"
        "buffer_store_dwordx2 v[60:61], v56, %[outr], 0 offen sc1\n\t"
        "s_and_b32 s66, s64, 0xff\n\t"
        "s_cbranch_scc0 .Ldone_%=\n\t"
        K2_ACC
        K2_NEXT_T2
        "s_sub_u32 s66, s66, 2\n\t"
        "s_mov_b32 s67, 0\n\t"
        "s_setpc_b64 s[62:63]\n"
        ".Legc_code_end:\n"
        K2_LOOP(sf, K2_SAFE)
        ".Lredo_%=:\n\t"
        "s_waitcnt vmcnt(0)\n\t"
        "s_mov_b64 s[74:75], -1\n\t"
        "s_mov_b32 s70, 1\n\t"
        "s_branch .Lstart_%=\n"
        ".Ldone_%=:\n\t"
        "s_waitcnt vmcnt(0) lgkmcnt(0)\n\t"
        :
        : [slow] "s"(slow), [lens] "s"(lens), [tot] "s"(tot), [vop] "v"(vop), [vep] "v"(vep),
          [ordb] "v"(ordb), [evo] "i"(K2_EV), [m0] "s"(vm[0]), [m1] "s"(vm[1]), [m2] "s"(vm[2]), [m3] "s"(vm[3]),
          [q0] "v"(p8[0]), [q1] "v"(p8[1]), [q2] "v"(p8[2]), [q3] "v"(p8[3]), "{v[108:111]}"(re[0]), "{v[112:115]}"(re[1]), [t1r] "s"(t1r), [t2r] "s"(t2r), [outr] "s"(outr), "{v[8:11]}"(asel), "{v[14:15]}"(cfv), "{v[50:51]}"(lo16),
          "{v7}"(olane)
        : "v18", "v19", "v20", "v21", "v22", "v23", "v24", "v25", "v26", "v27", "v28", "v29",
          "v30", "v31", "v32", "v33", "v34", "v35", "v36", "v37", "v38", "v39", "v40", "v41", "v42", "v43", "v44", "v45",
          "v46", "v47", "v48", "v49", "v52", "v53", "v54", "v56", "v57", "v58", "v59", "v60", "v61", "v62", "v63",
          "v64", "v65", "v66", "v67", "v68", "v69", "v70", "v71", "v72", "v73", "v74", "v75", "v76", "v77", "v78", "v79",
          "v80", "v81", "v82", "v83", "v84", "v85", "v86", "v87", "v88", "v89", "v90", "v91", "v92", "v93", "v94", "v95",
          "v96", "v97", "v98", "v99", "v100", "v101", "v102", "v103", "v104", "v105", "v106", "v107",
          "s60", "s61", "s62", "s63", "s64", "s65", "s66", "s67", "s68", "s69", "s70", "s71", "s72", "s73", "s74", "s75",
          "vcc", "scc", "memory");
    if (slow) k2_slow(smem, T1, T2, gw, ea, src, dst, out, N, E, s, node0);
}

}

extern "C" void kernel_launch(void* const* d_in, const int* in_sizes, int n_in,
                              void* d_out, int out_size, void* d_ws, size_t ws_size,
                              hipStream_t stream) {
    const uint16_t* x  = (const uint16_t*)d_in[0];
    const uint16_t* ea = (const uint16_t*)d_in[1];
    const int* src     = (const int*)d_in[2];
    const int* dst     = (const int*)d_in[3];
    const uint16_t* uw = (const uint16_t*)d_in[4];
    const uint16_t* vw = (const uint16_t*)d_in[5];
    const uint16_t* gw = (const uint16_t*)d_in[6];
    const int N = in_sizes[0] / D;
    const int E = in_sizes[2];
    const int nrb   = (N + BM - 1) / BM;
    const int nbins = (N + NBN - 1) / NBN;
    int nchunks = NCH;
    const int slots = nrb + nchunks / 8;
    int ec = (E + NCH * 32 - 1) / (NCH * 32) * 32;
    const int force_slow = (nbins > BINMAX || ec > RMAX * 2048) ? 1 : 0;
    if (force_slow) { nchunks = 0; ec = 0; }

    char* ws = (char*)d_ws;
    const size_t tb = ((size_t)NS * N * FS * 4 + 255) & ~(size_t)255;
    unsigned* T1 = (unsigned*)ws;
    unsigned* T2 = (unsigned*)(ws + tb);
    unsigned* recs = (unsigned*)(ws + 2 * tb);
    unsigned* tab = (unsigned*)(ws + 2 * tb + (((size_t)NCH * ec * 12 + 255) & ~(size_t)255));

    (void)hipFuncSetAttribute((const void*)egc_k1, hipFuncAttributeMaxDynamicSharedMemorySize, K1_LDS);
    egc_k1<<<slots * 8, 512, K1_LDS, stream>>>(x, ea, src, dst, uw, vw, gw, T1, T2, recs, tab,
                                                 N, E, nrb, nbins, ec, nchunks, (N + nrb - 1) / nrb, NBK * 65536 / N);
    egc_k2<<<((nbins + 1) / 2) * 8, 512, 0, stream>>>(T1, T2, recs, tab, gw, ea, src, dst, (uint16_t*)d_out,
                                                      N, E, nchunks, nbins, ec, force_slow);
}
```

```cpp
#include <hip/hip_runtime.h>
#include <hip/hip_bf16.h>
#include <stdint.h>

typedef float    f32x4 __attribute__((ext_vector_type(4)));
typedef float    f32x2 __attribute__((ext_vector_type(2)));
typedef short    s16x4 __attribute__((ext_vector_type(4)));
typedef __bf16   bf16x8 __attribute__((ext_vector_type(8)));
typedef __bf16   bf16x2 __attribute__((ext_vector_type(2)));
typedef unsigned u32x4 __attribute__((ext_vector_type(4)));
typedef unsigned u32x2 __attribute__((ext_vector_type(2)));
typedef __amdgpu_buffer_rsrc_t rsrc_t;
#define LDSP __attribute__((address_space(3)))

namespace {

constexpr int D     = 256;
constexpr int FS    = 64;
constexpr int NS    = 4;
constexpr int BM    = 384;
constexpr int NB    = 80;
constexpr int NBN   = 79;
constexpr int NCH   = 40;
constexpr int TSTR  = 64;
constexpr int RMAX  = 2;
constexpr int BINMAX = 256;
constexpr int SLOTS = 8;
constexpr int TPC   = 12;
constexpr int NBK   = 4;
constexpr int LSTR  = 76;
constexpr int MAXT  = 62;
constexpr unsigned MINL = 8;
constexpr float NLOG2E = -1.4426950408889634f;
constexpr unsigned SENT = 0x00fff000u;
constexpr unsigned OOB  = 0x7ffffff0u;
constexpr int AUX_WT = 16;

__device__ __forceinline__ rsrc_t mk_rsrc(const void* p, unsigned bytes) {
    return __builtin_amdgcn_make_buffer_rsrc((void*)p, (short)0, (int)bytes, 0x00020000);
}
__device__ __forceinline__ void dma16(rsrc_t r, unsigned voff, unsigned soff, unsigned lds) {
    asm volatile("s_mov_b32 m0, %0\n\ts_nop 0\n\tbuffer_load_dwordx4 %1, %2, %3 offen lds"
                 :: "s"(lds), "v"(voff), "s"(r), "s"(soff) : "m0", "memory");
}
#define WAIT_V(n) asm volatile("s_waitcnt vmcnt(%0)" :: "i"(n) : "memory")
#define WAIT_L0() asm volatile("s_waitcnt lgkmcnt(0)" ::: "memory")
#define BAR() __builtin_amdgcn_s_barrier()

__device__ __forceinline__ unsigned wave_scan(unsigned v) {
    v += __builtin_amdgcn_update_dpp(0u, v, 0x111, 0xf, 0xf, false);
    v += __builtin_amdgcn_update_dpp(0u, v, 0x112, 0xf, 0xf, false);
    v += __builtin_amdgcn_update_dpp(0u, v, 0x114, 0xf, 0xf, false);
    v += __builtin_amdgcn_update_dpp(0u, v, 0x118, 0xf, 0xf, false);
    v += __builtin_amdgcn_update_dpp(0u, v, 0x142, 0xa, 0xf, false);
    v += __builtin_amdgcn_update_dpp(0u, v, 0x143, 0xc, 0xf, false);
    return v;
}
__device__ __forceinline__ unsigned pack_bf16(float lo, float hi) {
    f32x2 p = {lo, hi};
    return __builtin_bit_cast(unsigned, __builtin_convertvector(p, bf16x2));
}
__device__ __forceinline__ float bf_lo(unsigned v) { return __builtin_bit_cast(float, v << 16); }
__device__ __forceinline__ float bf_hi(unsigned v) { return __builtin_bit_cast(float, v & 0xffff0000u); }

__device__ __forceinline__ unsigned pack_t1(float xb, float hv) {
    asm("" : "+v"(xb), "+v"(hv));
    const float p = xb * NLOG2E;
    unsigned P;
    asm("v_cvt_pk_bf16_f32 %0, %1, -%1" : "=v"(P) : "v"(p));
    float m = __builtin_fabsf(hv) + 1.17549435e-38f;
    asm("" : "+v"(m));
    const unsigned t = __builtin_bit_cast(unsigned, m) - (unsigned)((int)P >> 16);
    unsigned X = (t & 0xffff0000u) | (P & 0xffffu);
    asm("v_and_or_b32 %0, %1, %2, %0" : "+v"(X) : "v"(hv), "s"(0x80000000u));
    return X;
}

constexpr int K1_W   = 98304;
constexpr int K1_LDS = 163840;
__host__ __device__ constexpr int K1_R(int r) { return r == 5 ? 0 : r * 32768; }

__device__ __forceinline__ void k1_gemm(char* smem, const unsigned lds0, const int lane, const int w, const unsigned g0,
                                        const unsigned xvo, const rsrc_t xr, const rsrc_t tr) {
    const int g = lane >> 4, n = lane & 15;
    const int fh = w & 1, rg = w >> 1;
    const unsigned ldw = lds0 + w * 1024;
    unsigned gr[6];
#pragma unroll
    for (int r = 0; r < 6; ++r) gr[r] = (g0 >> (4 * r)) & 15u;
#define K1_LDX(r, i) dma16(xr, xvo, gr[r] * 32768 + (i) * 8192, ldw + K1_R(r) + (i) * 8192)

    const f32x4 zero4 = {0.f, 0.f, 0.f, 0.f};
    bf16x8 wf[8][4];
    bf16x8 xq[8];
    f32x4 acc[2][4];
    const unsigned fr = ((n >> 3) & 1) | ((g & 1) << 1);
    const char* wp0 = smem + K1_W + (8 * g + (n >> 2)) * 128 + (((2 * fh) ^ fr) << 5) + 8 * (n & 3);
    const char* wp1 = smem + K1_W + (8 * g + (n >> 2)) * 128 + (((2 * fh + 1) ^ fr) << 5) + 8 * (n & 3);
    unsigned xa[3][4];
#pragma unroll
    for (int k = 0; k < 4; ++k) {
        xa[0][k] = lds0 + n * 512 + rg * 8192 + (((4 * k + g) ^ n) << 4);
        xa[1][k] = xa[0][k] + 65536; xa[2][k] = xa[0][k] + 131072;
    }
    const unsigned ovo = g * 1024 + fh * 128 + n * 8 + rg * 4096;
#define K1_RDW(ks) do { _Pragma("unroll") for (int mf = 0; mf < 4; ++mf) {                                   \
        const char* wp = ((mf & 1) ? wp1 : wp0) + (ks) * 8192 + (mf >> 1) * 4096;                           \
        u32x2 l2 = __builtin_bit_cast(u32x2, __builtin_amdgcn_ds_read_tr16_b64_v4i16((LDSP s16x4*)wp));      \
        u32x2 h2 = __builtin_bit_cast(u32x2, __builtin_amdgcn_ds_read_tr16_b64_v4i16((LDSP s16x4*)(wp + 512))); \
        wf[ks][mf] = __builtin_bit_cast(bf16x8, u32x4{l2.x, l2.y, h2.x, h2.y}); } } while (0)
#define K1_RDX(r, ks) xq[ks] = *(const LDSP bf16x8*)(size_t)(xa[K1_R(r) >> 16][(ks) & 3] + (K1_R(r) & 65535) + ((ks) >> 2) * 256)
#define K1_MM(p, ks) do { _Pragma("unroll") for (int m = 0; m < 4; ++m)                                      \
        acc[p][m] = __builtin_amdgcn_mfma_f32_16x16x32_bf16(xq[ks], wf[ks][m], (ks) ? acc[p][m] : zero4, 0, 0, 0); } while (0)
#define SB() __builtin_amdgcn_sched_barrier(0)

    WAIT_V(4); BAR();
#pragma unroll
    for (int ks = 0; ks < 6; ++ks) K1_RDW(ks);
#pragma unroll
    for (int i = 0; i < 4; ++i) K1_LDX(1, i);
    K1_RDW(6); K1_RDW(7);
    SB();
    WAIT_L0(); WAIT_V(4); BAR();
#pragma unroll
    for (int ks = 0; ks < 8; ++ks) K1_RDX(0, ks);
    SB();
#pragma unroll
    for (int ks = 0; ks < 8; ++ks) {
        if (ks == 6) {
            WAIT_L0(); WAIT_V(12); BAR();
#pragma unroll
            for (int k = 0; k < 6; ++k) K1_RDX(1, k);
        }
        K1_MM(0, ks);
        if (ks >= 6) K1_RDX(1, ks);
        if (ks < 6) { K1_LDX(2 + (ks >> 1), (ks & 1) * 2); K1_LDX(2 + (ks >> 1), (ks & 1) * 2 + 1); }
        SB();
    }
#pragma unroll
    for (int r = 1; r < 6; ++r) {
        const int p = r & 1, q = p ^ 1;
        const unsigned vo = ovo + gr[r - 1] * 16384;
        u32x2 o;
        const bool bar = (r == 1 || r == 3);
#pragma unroll
        for (int ks = 0; ks < 8; ++ks) {
            if (bar && ks == 4) {
                WAIT_V(r == 1 ? 6 : 8); BAR();
#pragma unroll
                for (int k = 0; k < 4; ++k) K1_RDX(r + 1, k);
            }
            K1_MM(p, ks);
            if (r < 5 && (!bar || ks >= 4)) K1_RDX(r + 1, ks);
            o[ks & 1] = pack_t1(acc[q][ks & 1][ks >> 1], acc[q][2 + (ks & 1)][ks >> 1]);
            if (ks & 1) __builtin_amdgcn_raw_buffer_store_b64(o, tr, vo + (ks >> 1) * 256, 0, AUX_WT);
            if (r == 1 && ks < 4) K1_LDX(5, ks);
#pragma unroll
            for (int i = 0; i < 4; ++i) {
                __builtin_amdgcn_sched_group_barrier(0x008, 1, 0);
                __builtin_amdgcn_sched_group_barrier(0x002, 2, 0);
            }
            SB();
        }
    }
#pragma unroll
    for (int r = 0; r < 4; ++r) {
        const u32x2 o = {pack_t1(acc[1][0][r], acc[1][2][r]), pack_t1(acc[1][1][r], acc[1][3][r])};
        __builtin_amdgcn_raw_buffer_store_b64(o, tr, ovo + gr[5] * 16384 + r * 256, 0, AUX_WT);
    }
#undef K1_LDX
}

__global__ __launch_bounds__(512, 2)
void egc_k1(const uint16_t* __restrict__ x, const uint16_t* __restrict__ ea,
            const int* __restrict__ src, const int* __restrict__ dst,
            const uint16_t* __restrict__ uw, const uint16_t* __restrict__ vw,
            const uint16_t* __restrict__ gw,
            unsigned* __restrict__ T1, unsigned* __restrict__ T2,
            unsigned* __restrict__ recs, unsigned* __restrict__ tab,
            int N, int E, int nrb, int nbins, int ec, int nchunks, int rows, int bmul)
{
    extern __shared__ __attribute__((aligned(16))) char smem[];
    const int b   = blockIdx.x;
    const int slot = b >> 3;
    const int item = (b & 7) * nrb + slot;
    const int rb  = item >> 3;
    const int ct  = item & 7;
    const int tid = threadIdx.x;
    const int lane = tid & 63;
    const int w = __builtin_amdgcn_readfirstlane(tid >> 6);
    asm volatile(".Legc_code_begin:" :: "s"(x), "s"(ea), "s"(src), "s"(dst), "s"(uw), "s"(vw), "s"(gw), "s"(T1), "s"(T2), "s"(recs), "s"(tab),
                       "s"(N), "s"(E), "s"(nrb), "s"(nbins), "s"(ec), "s"(nchunks), "s"(rows), "s"(bmul));

    const int s = ct >> 1;
    const int row0 = rb * rows;
    const unsigned nr = (unsigned)max(min(rows, N - row0), 0);
    const unsigned lds0 = (unsigned)(size_t)(LDSP char*)smem;
    const rsrc_t xr = mk_rsrc(x + (size_t)row0 * D, nr * 512u);
    const unsigned rl = 2 * w + (lane >> 5);
    const unsigned xvo = rl * 512 + (((lane & 31) ^ rl) << 4);
    const unsigned g0 = (unsigned)(0x2154321543210ull >> (4 * ct)) & ~15u;
    {
        const uint16_t* wsel = (ct & 1) ? (w < 4 ? gw : uw) : (w < 4 ? gw + D * D : vw);
        const unsigned f = ((lane >> 4) & 1) | ((w & 1) << 1);
        const unsigned wvo = (lane >> 3) * 512 + s * 128 + ((((lane >> 1) & 3) ^ f) << 5) + (lane & 1) * 16 + (w & 3) * 4096;
        unsigned t0, t1;
#define K1_W1 "s_lshl_b32 %1, %0, 1\n\ts_add_u32 m0, %0, %[lw]\n\ts_add_u32 %0, %0, 0x2000\n\t"                  \
              "buffer_load_dwordx4 %[wvo], %[wr], %1 offen lds\n\ts_and_b32 %0, %0, 0xe000\n\t"
#define K1_X1(O) "s_add_u32 m0, %[lx], " #O "\n\ts_add_u32 %1, %[gx], " #O "\n\tbuffer_load_dwordx4 %[xvo], %[xr], %1 offen lds\n\t"
        asm volatile("s_cmp_ge_i32 %[slot], %[nrb]\n\t"
                     "s_cbranch_scc1 .Lk1skip_%=\n\t"
                     "s_mov_b32 %0, %[k0]\n\t"
                     K1_W1 K1_W1 K1_W1 K1_W1 K1_W1 K1_W1 K1_W1 K1_W1
                     "s_barrier\n\t"
                     K1_X1(0) K1_X1(0x2000) K1_X1(0x4000) K1_X1(0x6000)
                     ".Lk1skip_%=:"
                     : "=&s"(t0), "=&s"(t1)
                     : [slot] "s"(slot), [nrb] "s"(nrb), [k0] "s"((rb & 3) * 16384), [lw] "s"(lds0 + w * 1024 + K1_W),
                       [wvo] "v"(wvo), [wr] "s"(mk_rsrc(wsel, D * D * 2)), [lx] "s"(lds0 + w * 1024), [gx] "s"((g0 & 15u) * 32768),
                       [xvo] "v"(xvo), [xr] "s"(xr)
                     : "m0", "scc", "memory");
    }
    if (slot < nrb) {
        k1_gemm(smem, lds0, lane, w, g0, xvo, xr,
                mk_rsrc(((ct & 1) ? T2 : T1) + ((size_t)s * N + row0) * FS, nr * 256u));
        return;
    }
    {
        const int c = (slot - nrb) * 8 + (b & 7);
        {
            unsigned long long pc;
            unsigned len, back;
            asm volatile("s_getpc_b64 %0\n.Legc_code_here:\n\ts_mov_b32 %1, .Legc_code_end-.Legc_code_begin\n\t"
                         "s_mov_b32 %2, .Legc_code_here-.Legc_code_begin" : "=s"(pc), "=s"(len), "=s"(back));
            asm volatile("s_mov_b32 m0, %0\n\ts_nop 0\n\tbuffer_load_dword %1, %2, 0 offen lds"
                         :: "s"(lds0 + 131072 + w * 1024), "v"(tid * 64),
                            "s"(mk_rsrc((const void*)(pc - back), (back > 65536u || len > 65536u) ? 0u : len)) : "m0", "memory");
        }
        if (c == 0 && tid < 256) {
            const int g = lane >> 4, n = lane & 15;
            const uint16_t* cw = gw + 2 * D * D + w * FS + (n >> 3) * 32 + (n & 7) * 2 + (g >> 1) + (g & 1) * 16;
            const u32x2 cf = {pack_bf16(bf_lo(cw[0]) * NLOG2E, bf_lo(cw[D]) * NLOG2E),
                              pack_bf16(bf_lo(cw[2 * D]) * NLOG2E, bf_lo(cw[3 * D]) * NLOG2E)};
            __builtin_amdgcn_raw_buffer_store_b64(cf, mk_rsrc(tab + BINMAX * TSTR, 2048), tid * 8, 0, AUX_WT);
        }
        if (c >= nchunks) return;
        unsigned* cw  = (unsigned*)smem;
        unsigned* wt  = cw + 8 * BINMAX;
        unsigned* rme = wt + 16;
        u32x2*    rev = (u32x2*)(rme + RMAX * 2048);
        const int e0 = c * ec;
        const int eb = ((tid >> 4) * NCH + c) * 32 + (tid & 15) * 2;
        constexpr int RS = 32 * NCH * 32, NR = 2 * RMAX;
        const rsrc_t dr = mk_rsrc(dst, E * 4u), sr = mk_rsrc(src, E * 4u), er = mk_rsrc(ea, E * 8u);
        u32x2 dv[NR], sv[NR];
        u32x4 ev[NR];
#pragma unroll
        for (int r = 0; r < NR; ++r) dv[r] = __builtin_amdgcn_raw_buffer_load_b64(dr, eb * 4, r * RS * 4, 2);
        __builtin_amdgcn_sched_barrier(0);
#pragma unroll
        for (int r = 0; r < NR; ++r) {
            ev[r] = __builtin_amdgcn_raw_buffer_load_b128(er, eb * 8, r * RS * 8, 2);
            sv[r] = __builtin_amdgcn_raw_buffer_load_b64(sr, eb * 4, r * RS * 4, 2);
        }
        ((u32x4*)cw)[tid] = u32x4{0u, 0u, 0u, 0u};
        __builtin_amdgcn_sched_barrier(0);
        __syncthreads();
        unsigned* cmine = cw + w * BINMAX;
        unsigned rk[NR][2], bn[NR][2];
#pragma unroll
        for (int r = 0; r < NR; ++r)
#pragma unroll
            for (int k = 0; k < 2; ++k) {
                bn[r][k] = __umul24(dv[r][k], 53093u) >> 22;
                rk[r][k] = 0;
                if (r * RS + eb + k < E) rk[r][k] = atomicAdd(&cmine[bn[r][k]], 1u);
            }
        __syncthreads();
        {
            unsigned pre[8], t = 0, inc = 0;
            if (tid < BINMAX) {
#pragma unroll
                for (int k = 0; k < 8; ++k) { pre[k] = t; t += cw[k * BINMAX + tid]; }
                inc = wave_scan(t);
                if (lane == 63) wt[w] = inc;
            }
            __syncthreads();
            if (tid < BINMAX) {
                const u32x4 w4 = *(const u32x4*)wt;
                const unsigned st = inc - t + (w > 0 ? w4[0] : 0u) + (w > 1 ? w4[1] : 0u) + (w > 2 ? w4[2] : 0u);
#pragma unroll
                for (int k = 0; k < 8; ++k) cw[k * BINMAX + tid] = st + pre[k];
                if (tid < nbins) __builtin_amdgcn_raw_buffer_store_b32(st | (t << 16), mk_rsrc(tab, BINMAX * TSTR * 4), (tid * TSTR + c) * 4, 0, AUX_WT);
            }
        }
        __syncthreads();
#pragma unroll
        for (int r = 0; r < NR; ++r)
#pragma unroll
            for (int k = 0; k < 2; ++k) rk[r][k] += cmine[bn[r][k]];
#pragma unroll
        for (int r = 0; r < NR; ++r)
#pragma unroll
            for (int k = 0; k < 2; ++k)
                if (r * RS + eb + k < E) {
                    rev[rk[r][k]] = u32x2{ev[r][2 * k], ev[r][2 * k + 1]};
                    rme[rk[r][k]] = (sv[r][k] << 16) + (dv[r][k] - bn[r][k] * NBN + (__umul24(sv[r][k], bmul) >> 16) * NB) * 16u;
                }
        __syncthreads();
        const rsrc_t omr = mk_rsrc(recs + e0, ec * 4), oer = mk_rsrc(recs + NCH * ec + e0 * 2, ec * 8);
#pragma unroll
        for (int i = 0; i < RMAX * 3; ++i) {
            const u32x4 v = ((const u32x4*)rme)[tid + 512 * i];
            __builtin_amdgcn_raw_buffer_store_b128(v, i < RMAX ? omr : oer, tid * 16 + (i < RMAX ? i : i - RMAX) * 8192, 0, AUX_WT);
        }
        __builtin_amdgcn_s_waitcnt(0);
    }
}

constexpr int K2_OFFS = 0;
constexpr int K2_EV   = K2_OFFS + 32 * LSTR * 4;
constexpr int K2_CNT  = K2_EV + 33 * LSTR * 8;
constexpr int K2_HIST = K2_CNT + NBK * NB * 16;
constexpr int K2_ORD  = K2_HIST + 256;
constexpr int K2_LEN  = K2_ORD + 128;
constexpr int K2_FLAG = K2_LEN + 32;
constexpr int K2_PF   = K2_FLAG + 16;
constexpr int K2_LDS  = K2_PF + 8192;
static_assert((K2_PF - K2_EV) % 16 == 0 && K2_EV % 16 == 0, "LDS map");

__device__ __forceinline__ u32x2 silu_pack(f32x4 y) {
    f32x4 o;
#pragma unroll
    for (int r = 0; r < 4; ++r) {
        float t = __builtin_amdgcn_exp2f(y[r] * NLOG2E);
        o[r] = y[r] * __builtin_amdgcn_rcpf(1.0f + t);
    }
    return u32x2{pack_bf16(o[0], o[1]), pack_bf16(o[2], o[3])};
}

__device__ __forceinline__ void k2_slow(char* smem, const unsigned* __restrict__ T1, const unsigned* __restrict__ T2,
                                        const uint16_t* __restrict__ gw, const uint16_t* __restrict__ ea,
                                        const int* __restrict__ src, const int* __restrict__ dst,
                                        uint16_t* __restrict__ out, int N, int E, int s, int node0) {
    float* accs = (float*)smem;
    unsigned* queue = (unsigned*)(smem + NB * FS * 4);
    static_assert(NB * FS * 4 + 2048 <= K2_CNT, "slow-path LDS");
    const unsigned* t2g = T2 + ((size_t)s * N + node0) * FS;
    const int tid = threadIdx.x, f = tid & 63, w = tid >> 6;
    __syncthreads();
    for (int i = tid; i < NB * FS; i += 512) accs[i] = 0.f;
    const uint16_t* cf = gw + (size_t)2 * D * D + s * FS + (f & 32) + (f & 1) * 16 + ((f & 31) >> 1);
    const float c0 = bf_lo(cf[0]) * NLOG2E, c1 = bf_lo(cf[D]) * NLOG2E;
    const float c2 = bf_lo(cf[2 * D]) * NLOG2E, c3 = bf_lo(cf[3 * D]) * NLOG2E;
#pragma unroll 1
    for (int eb = 0; eb < E; eb += 512) {
        __syncthreads();
        const int e = eb + tid;
        const int dn = (e < E) ? dst[e] - node0 : -1;
        queue[tid] = (dn >= 0 && dn < NBN) ? (unsigned)dn : 0xffffffffu;
        __syncthreads();
#pragma unroll 1
        for (int q0 = 0; q0 < 512; q0 += 64) {
            const unsigned qv = queue[q0 + f];
            unsigned long long m = __ballot(qv != 0xffffffffu && (qv & 7u) == (unsigned)w);
            while (m) {
                const int l = __builtin_ctzll(m);
                m &= m - 1;
                const int ee = eb + q0 + l;
                const int d2 = __builtin_amdgcn_readlane(qv, l);
                const unsigned tv = T1[((size_t)s * N + src[ee]) * FS + f];
                const u32x2 e2 = *(const u32x2*)(ea + (size_t)ee * 4);
                const float z = bf_lo(t2g[d2 * FS + f]) + bf_lo(tv) + bf_lo(e2.x) * c0 + bf_hi(e2.x) * c1
                                + bf_lo(e2.y) * c2 + bf_hi(e2.y) * c3;
                accs[d2 * FS + f] += __builtin_bit_cast(float, tv) * __builtin_amdgcn_rcpf(1.0f + __builtin_amdgcn_exp2f(z));
            }
        }
    }
    __syncthreads();
#pragma unroll 1
    for (int p = tid; p < NBN * 16; p += 512) {
        const int r = p >> 4, q4 = (p & 15) * 4;
        if (node0 + r < N) {
            f32x4 y;
#pragma unroll
            for (int k = 0; k < 4; ++k) {
                const int sl = r * FS + (q4 & 32) + ((q4 & 15) + k) * 2 + ((q4 >> 4) & 1);
                y[k] = accs[sl] + __builtin_bit_cast(float, t2g[sl]);
            }
            *(u32x2*)(out + (size_t)(node0 + r) * D + s * FS + q4) = silu_pack(y);
        }
    }
}

__global__ __launch_bounds__(512, 4)
void egc_k2(const unsigned* __restrict__ T1, const unsigned* __restrict__ T2,
            const unsigned* __restrict__ recs, const unsigned* __restrict__ tab,
            const uint16_t* __restrict__ gw, const uint16_t* __restrict__ ea,
            const int* __restrict__ src, const int* __restrict__ dst,
            uint16_t* __restrict__ out, int N, int E, int nchunks, int nbins, int ec, int force_slow)
{
    __shared__ __attribute__((aligned(16))) char smem[K2_LDS];
    unsigned* hist = (unsigned*)(smem + K2_HIST);
    unsigned* flag = (unsigned*)(smem + K2_FLAG);

    asm volatile("" :: "s"(T1), "s"(T2), "s"(recs), "s"(tab), "s"(out), "s"(N), "s"(nchunks),
                       "s"(nbins), "s"(ec), "s"(force_slow));
    const int b   = blockIdx.x;
    const int s   = (b & 7) >> 1;
    const int hb  = (nbins + 1) >> 1;
    const int bin = (b & 1) * hb + (b >> 3);
    const int tid = threadIdx.x;
    const int lane = tid & 63;
    const int w = __builtin_amdgcn_readfirstlane(tid >> 6);
    const int g = lane >> 4, n = lane & 15;
    const int node0 = bin * NBN;
    const unsigned lds0 = (unsigned)(size_t)(LDSP char*)smem;
    const rsrc_t t1r = mk_rsrc(T1 + (size_t)s * N * FS, (unsigned)N * 256u);
    __builtin_amdgcn_s_setprio(3);

    const unsigned c = __umul24(tid, 5462u) >> 16, j = tid - c * TPC;
    unsigned te;
    asm volatile("buffer_load_dword %0, %1, %2, 0 offen"
                 : "=&v"(te) : "v"(c * 4), "s"(mk_rsrc(tab + bin * TSTR, bin < nbins ? nchunks * 4 : 0)) : "memory");
    dma16(t1r, (b >> 3) * 8192u + tid * 16u, 0, lds0 + K2_PF + w * 1024);
    {
        const u32x4 sv = {SENT, SENT, SENT, SENT}, zv = {0u, 0u, 0u, 0u};
        u32x4* po = (u32x4*)(smem + K2_OFFS);
        constexpr int NO = 32 * LSTR / 4, NZ = (K2_PF - K2_EV) / 16;
        po[tid] = sv;
        if (tid < NO - 512) po[tid + 512] = sv;
        u32x4* pz = (u32x4*)(smem + K2_EV);
#pragma unroll
        for (int i = 0; i < NZ / 512; ++i) pz[tid + 512 * i] = zv;
        if (tid < NZ % 512) pz[tid + NZ / 512 * 512] = zv;
    }
    u32x4 asel;
#pragma unroll
    for (int k = 0; k < 4; ++k) asel[k] = (n == 4 * g + k) ? 0x3f80u : 0u;
    u32x2 lo16 = {(unsigned)n * 16u, (unsigned)n * 16u};
    unsigned vop = lds0 + K2_OFFS + (w * 4 + g) * (LSTR * 4);
    unsigned vep = lds0 + K2_EV + (((n & 3) == g) ? w * 4 + (n >> 2) : 32) * (LSTR * 8);
    unsigned olane = (n >> 3) * 64 + (n & 6) * 4 + (n & 1) * 32;
    unsigned j4 = 4 * j, rb0 = __umul24(c, ec) + j4;
    const rsrc_t mr = mk_rsrc(recs, NCH * 4u * ec), er = mk_rsrc(recs + NCH * ec, NCH * 8u * ec);
    asm volatile("s_waitcnt vmcnt(1)" : "+v"(te), "+v"(asel), "+v"(lo16), "+v"(vop), "+v"(vep), "+v"(olane), "+v"(j4), "+v"(rb0)
                 : "s"(mr), "s"(er) : "memory");
    const int nc = (int)(te >> 16) - (int)j4;
    u32x4 rm[2], re[4];
    const unsigned r0 = nc > 0 ? (rb0 + (te & 0xffffu)) * 4u : OOB / 2;
    asm volatile("buffer_load_dwordx4 %0, %1, %2, 0 offen" : "=&v"(rm[0]) : "v"(r0), "s"(mr) : "memory");
    __builtin_amdgcn_sched_barrier(0);
    unsigned long long vm[SLOTS];
#pragma unroll
    for (int i = 0; i < SLOTS; ++i) vm[i] = __builtin_amdgcn_ballot_w64(nc > (i >> 2) * (4 * TPC) + (i & 3));
    const bool big = vm[4] != 0;
    asm volatile("s_cmp_eq_u64 %3, 0\n\t"
                 "s_cbranch_scc1 .Lnb_%=\n\t"
                 "buffer_load_dwordx4 %0, %1, %2, 0 offen offset:192\n"
                 ".Lnb_%=:"
                 : "=&v"(rm[1]) : "v"(r0), "s"(mr), "s"(vm[4]) : "scc", "memory");
    if (bin >= nbins) { WAIT_V(0); return; }
    __syncthreads();
    if (nc > TPC * SLOTS || force_slow) flag[0] = 1u;
    asm volatile("s_waitcnt vmcnt(0)" : "+v"(rm[0]), "+v"(rm[1]) :: "memory");
    u32x2 cfv;
    asm volatile("s_cmp_eq_u64 %4, 0\n\t"
                 "s_cbranch_scc1 .Lnb_%=\n\t"
                 "buffer_load_dwordx4 %0, %2, %3, 0 offen offset:384\n\t"
                 "buffer_load_dwordx4 %1, %2, %3, 0 offen offset:400\n"
                 ".Lnb_%=:"
                 : "=&v"(re[2]), "=&v"(re[3]) : "v"(r0 * 2), "s"(er), "s"(vm[4]) : "scc", "memory");
    asm volatile("buffer_load_dwordx4 %0, %3, %5, 0 offen\n\t"
                 "buffer_load_dwordx4 %1, %3, %5, 0 offen offset:16\n\t"
                 "buffer_load_dwordx2 %2, %4, %6, 0 offen"
                 : "=&{v[108:111]}"(re[0]), "=&{v[112:115]}"(re[1]), "=&{v[14:15]}"(cfv)
                 : "v"(r0 * 2), "v"(lane * 8), "s"(er), "s"(mk_rsrc(tab + BINMAX * TSTR + s * 128, 512)) : "memory");
    unsigned ca[SLOTS], rk[SLOTS];
#define K2_CA(I) (lds0 + K2_CNT + (w & 4) * 2 + (rm[(I) >> 2][(I) & 3] & 0xffffu))
#pragma unroll
    for (int i = 0; i < 4; ++i) ca[i] = K2_CA(i);
#define K2_AT(R, A, M) "s_mov_b64 exec, %[" #M "]\n\tds_add_rtn_u32 %[" #R "], %[" #A "], %[one]\n\t"
#define K2_AT4(O) asm volatile(K2_AT(r0, a0, m0) K2_AT(r1, a1, m1) K2_AT(r2, a2, m2) K2_AT(r3, a3, m3)                   \
                 "s_mov_b64 exec, -1\n\ts_waitcnt lgkmcnt(0)"                                                            \
                 : [r0] "=&v"(rk[O]), [r1] "=&v"(rk[O + 1]), [r2] "=&v"(rk[O + 2]), [r3] "=&v"(rk[O + 3])                \
                 : [a0] "v"(ca[O]), [a1] "v"(ca[O + 1]), [a2] "v"(ca[O + 2]), [a3] "v"(ca[O + 3]),                       \
                   [m0] "s"(vm[O]), [m1] "s"(vm[O + 1]), [m2] "s"(vm[O + 2]), [m3] "s"(vm[O + 3]),                       \
                   [one] "v"(1u << (8 * (w & 3))) : "memory")
    K2_AT4(0);
    asm volatile("" : "=v"(rk[4]), "=v"(rk[5]), "=v"(rk[6]), "=v"(rk[7]));
    if (big) {
#pragma unroll
        for (int i = 4; i < SLOTS; ++i) ca[i] = K2_CA(i);
        K2_AT4(4);
    }
    __syncthreads();
    if (w >= 4) {
#pragma unroll
        for (int i = 0; i < 2; ++i)
            dma16(t1r, (b >> 3) * 4096u + (tid - 256) * 16u, hb * 4096u * (i + 2), lds0 + K2_PF + w * 1024);
    }

    unsigned pre[2 * NBK], deg = 0, key = 0, rank = 0;
    if (tid < NB) {
        u32x4 f4[NBK];
#pragma unroll
        for (int k = 0; k < NBK; ++k) f4[k] = *(const u32x4*)(smem + K2_CNT + (k * NB + tid) * 16);
#pragma unroll
        for (int k = 0; k < NBK; ++k) {
            pre[2 * k] = deg;
            pre[2 * k + 1] = __builtin_amdgcn_sad_u8(f4[k][0], 0u, deg);
            deg = __builtin_amdgcn_sad_u8(f4[k][2], 0u, pre[2 * k + 1]);
        }
        key = 63u - min(deg, 63u);
        rank = (atomicAdd(&hist[key], 1u << (16 * w)) >> (16 * w)) & 0xffffu;
    }
    {
        unsigned long long bad = 0;
#pragma unroll
        for (int i = 0; i < 4; ++i) {
            rk[i] = (rk[i] >> (8 * (w & 3))) & 255u;
            bad |= __builtin_amdgcn_ballot_w64(rk[i] > 63u) & vm[i];
        }
        if (big) {
#pragma unroll
            for (int i = 4; i < SLOTS; ++i) {
                rk[i] = (rk[i] >> (8 * (w & 3))) & 255u;
                bad |= __builtin_amdgcn_ballot_w64(rk[i] > 63u) & vm[i];
            }
        }
        if (bad) flag[0] = 1u;
    }
    __syncthreads();
    unsigned wv = 0, pos = 0;
    if (w < 2) {
        const unsigned hv = hist[lane], h1 = hv >> 16, ht = (hv & 0xffffu) + h1;
        const unsigned ex = wave_scan(ht) - (w ? h1 : ht);
        rank += __builtin_amdgcn_ds_bpermute(key * 4, ex);
        pos = rank >> 4;
        wv = ((rank >> 2) & 3u) ^ (pos ? 3u : 0u);
        if (pos >= 2) { wv += 4; pos -= 2; }
        if (tid < NB) {
            smem[K2_ORD + wv * 16 + (rank & 3u) * 4 + pos] = (char)tid;
            if ((rank & 3u) == 0) smem[K2_LEN + wv * 4 + pos] = (char)min(max((deg + 1u) & ~1u, MINL), 254u);
        }
    }
    __syncthreads();
    if (tid < NB) {
        const unsigned l4 = *(const unsigned*)(smem + K2_LEN + wv * 4);
        const unsigned first = __builtin_amdgcn_sad_u8(l4 & ((1u << (8 * pos)) - 1u), 0u, 0u);
        const unsigned base = (wv * 4 + (rank & 3u)) * LSTR + first;
        if (first + max(deg, MINL) > MAXT) flag[0] = 1u;
#pragma unroll
        for (int k = 0; k < NBK; ++k) {
            *(unsigned*)(smem + K2_CNT + (k * NB + tid) * 16 + 4) = base + pre[2 * k];
            *(unsigned*)(smem + K2_CNT + (k * NB + tid) * 16 + 12) = base + pre[2 * k + 1];
        }
    }
    unsigned lens = __builtin_amdgcn_readfirstlane(*(const unsigned*)(smem + K2_LEN + w * 4));
    unsigned ordb = *(const unsigned*)(smem + K2_ORD + w * 16 + g * 4);
    const rsrc_t t2r = mk_rsrc(T2 + ((size_t)s * N + node0) * FS, (unsigned)(N - node0) * 256u);
    dma16(t2r, (ordb & 255u) * 256u + lo16[0], 0, lds0 + K2_PF + w * 1024);
    __syncthreads();

    u32x2 cs[SLOTS];
#pragma unroll
    for (int i = 0; i < 4; ++i) cs[i] = *(const LDSP u32x2*)(size_t)ca[i];
    const unsigned slow = __builtin_amdgcn_readfirstlane(flag[0]);
#pragma unroll
    for (int i = 0; i < SLOTS; ++i) vm[i] = slow ? 0ull : vm[i];
    unsigned p8[4];
    {
        const unsigned lowm = (1u << (8 * (w & 3))) - 1u;
#pragma unroll
        for (int i = 0; i < 4; ++i) {
            const unsigned p = __builtin_amdgcn_sad_u8(cs[i][0] & lowm, 0u, rk[i]) + cs[i][1];
            asm volatile("s_mov_b64 exec, %2\n\tds_write_b32 %0, %1 offset:%c3\n\ts_mov_b64 exec, -1"
                         :: "v"(p * 4), "v"((rm[0][i] >> 16) << 8), "s"(vm[i]), "i"(K2_OFFS) : "memory");
            p8[i] = p * 8;
        }
        if (big) {
            asm volatile("s_waitcnt vmcnt(3)" : "+v"(re[2]), "+v"(re[3]) :: "memory");
#pragma unroll
            for (int i = 4; i < SLOTS; ++i) cs[i] = *(const LDSP u32x2*)(size_t)K2_CA(i);
#pragma unroll
            for (int i = 4; i < SLOTS; ++i) {
                const unsigned p = __builtin_amdgcn_sad_u8(cs[i][0] & lowm, 0u, rk[i]) + cs[i][1];
                const u32x2 e2 = {re[i >> 1][(i & 1) * 2], re[i >> 1][(i & 1) * 2 + 1]};
                asm volatile("s_mov_b64 exec, %4\n\tds_write_b32 %0, %1 offset:%c5\n\tds_write_b64 %2, %3 offset:%c6\n\ts_mov_b64 exec, -1"
                             :: "v"(p * 4), "v"((rm[1][i & 3] >> 16) << 8), "v"(p * 8), "v"(e2), "s"(vm[i]), "i"(K2_OFFS), "i"(K2_EV) : "memory");
            }
        }
    }
    const rsrc_t outr = mk_rsrc(out + (size_t)node0 * D + s * FS, (unsigned)min(N - node0, NBN) * 512u - s * 128);
    WAIT_L0();
    __syncthreads();
    unsigned tot = (lens & 255) + ((lens >> 8) & 255) + ((lens >> 16) & 255);
    lens >>= 1;

#define V2(a, b) "v[" #a ":" #b "]"
#define K2_EXP2(a, b) "v_exp_f32 v" #a ", v" #a "\n\tv_exp_f32 v" #b ", v" #b "\n\t"
#define K2_RCP4(a, b, c, d) "v_rcp_f32 v" #a ", v" #a "\n\tv_rcp_f32 v" #b ", v" #b "\n\tv_rcp_f32 v" #c ", v" #c "\n\tv_rcp_f32 v" #d ", v" #d "\n\t"
#define K2_ADD1(x) "v_pk_add_f32 " x ", " x ", 1.0 op_sel_hi:[1,0]\n\t"
#define K2_FMA(d, x, y, z) "v_pk_fma_f32 " d ", " x ", " y ", " z "\n\t"
#define K2_RF(TL, TH, HL, HH)                                                     \
        K2_FMA("v[22:23]", "v[22:23]", TL, "v[22:23]") K2_FMA("v[24:25]", "v[24:25]", TH, "v[24:25]")         \
        K2_FMA("v[22:23]", HL, "v[104:105]", "v[22:23]") K2_FMA("v[24:25]", HH, "v[106:107]", "v[24:25]")     \
        K2_FMA("v[104:105]", "v[104:105]", TL, "v[104:105]") K2_FMA("v[106:107]", "v[106:107]", TH, "v[106:107]")
#define K2_FAST(A0, A1, A2, A3, B0, B1, B2, B3, HAL, HAH, HBL, HBH, ADD)          \
        K2_RF(V2(B0, B1), V2(B2, B3), HBL, HBH) ADD K2_RF(V2(A0, A1), V2(A2, A3), HAL, HAH)
#define K2_SAFE(A0, A1, A2, A3, B0, B1, B2, B3, HAL, HAH, HBL, HBH, ADD)          \
        K2_ADD1(V2(B0, B1)) K2_ADD1(V2(B2, B3)) K2_ADD1(V2(A0, A1)) K2_ADD1(V2(A2, A3)) \
        ADD                                                                       \
        K2_RCP4(A0, A1, A2, A3) K2_RCP4(B0, B1, B2, B3)                           \
        K2_FMA("v[22:23]", V2(A0, A1), HAL, "v[22:23]") K2_FMA("v[24:25]", V2(A2, A3), HAH, "v[24:25]") \
        K2_FMA("v[22:23]", V2(B0, B1), HBL, "v[22:23]") K2_FMA("v[24:25]", V2(B2, B3), HBH, "v[24:25]")
#define K2_RAL "v[38:39]"
#define K2_RAH "v[40:41]"
#define K2_RBL "v[42:43]"
#define K2_RBH "v[44:45]"
#define K2_YES(x) x
#define K2_NO(x) ""
#define K2_PAIR(LB, MATH, LD, VW, A0, A1, A2, A3, B0, B1, B2, B3, NA, NB_, H0, H1, H2, H3, H4, H5, H6, H7, DNA, DNB, EK, ERD, AL, AH, ADD, ARD) \
        "s_sub_u32 s66, s66, 1\n\t"                                               \
        "s_cbranch_scc1 .Lsw" #LB "_%=\n"                                          \
        ".Lrt" #LB "_%=:\n\t"                                                      \
        LD("s_waitcnt lgkmcnt(2)\n\t")                                            \
        "v_mfma_f32_16x16x16_bf16 " NA ", " EK##L ", v[14:15], v[18:21]\n\t"      \
        K2_EXP2(B0, B1)                                                           \
        "v_mfma_f32_16x16x16_bf16 " NB_ ", " EK##H ", v[14:15], v[18:21]\n\t"     \
        ERD                                                                       \
        K2_EXP2(B2, B3)                                                           \
        "s_waitcnt " VW "\n\t"                                                    \
        "v_mfma_f32_16x16x32_bf16 " NA ", v[8:11], " DNA ", " NA "\n\t"           \
        K2_EXP2(A0, A1)                                                           \
        "v_mfma_f32_16x16x32_bf16 " NB_ ", v[8:11], " DNB ", " NB_ "\n\t"         \
        K2_EXP2(A2, A3)                                                           \
        MATH(A0, A1, A2, A3, B0, B1, B2, B3, V2(H0, H1), V2(H2, H3), V2(H4, H5), V2(H6, H7), LD(ADD)) \
        LD("buffer_load_dwordx4 " V2(H0, H3) ", " AL ", %[t1r], 0 offen\n\t")     \
        LD("buffer_load_dwordx4 " V2(H4, H7) ", " AH ", %[t1r], 0 offen\n\t")     \
        LD(ARD)
#define K2_BODY(L, MATH, LD, W0, W1, W2, W3)                                      \
        K2_PAIR(L##0, MATH, LD, W0, 30, 31, 32, 33, 34, 35, 36, 37, "v[96:99]", "v[100:103]", 64, 65, 66, 67, 68, 69, 70, 71,   \
                "v[72:75]", "v[76:79]", K2_RB, K2_EB(48), "v46", "v47", K2_ADDA, K2_AA(48))                                \
        K2_PAIR(L##1, MATH, LD, W1, 96, 97, 98, 99, 100, 101, 102, 103, "v[30:33]", "v[34:37]", 72, 73, 74, 75, 76, 77, 78, 79, \
                "v[80:83]", "v[84:87]", K2_RA, LD(K2_EA(64)), "v48", "v49", K2_ADDB, K2_AB(56))                            \
        K2_PAIR(L##2, MATH, LD, W2, 30, 31, 32, 33, 34, 35, 36, 37, "v[96:99]", "v[100:103]", 80, 81, 82, 83, 84, 85, 86, 87,   \
                "v[88:91]", "v[92:95]", K2_RB, LD(K2_EB(80)), "v46", "v47", K2_ADDA, K2_AA(64))                            \
        K2_PAIR(L##3, MATH, LD, W3, 96, 97, 98, 99, 100, 101, 102, 103, "v[30:33]", "v[34:37]", 88, 89, 90, 91, 92, 93, 94, 95, \
                "v[64:67]", "v[68:71]", K2_RA, LD(K2_EA(96)), "v48", "v49", K2_ADDB, K2_AB(72))
#define K2_LOOP(L, MATH)                                                          \
        ".Lloop" #L "_%=:\n\t"                                                    \
        K2_BODY(L, MATH, K2_YES, "vmcnt(4)", "vmcnt(4)", "vmcnt(4)", "vmcnt(4)")  \
        "v_add_u32 v53, 64, v53\n\t"                                              \
        "v_add_u32 v52, 32, v52\n\t"                                              \
        "s_cmp_eq_u32 s71, 0\n\t"                                                 \
        "s_cbranch_scc1 .Lt2d" #L "_%=\n\t"                                       \
        "s_mov_b32 s71, 0\n\t"                                                    \
        K2_NEXT_T2                                                                \
        ".Lt2d" #L "_%=:\n\t"                                                     \
          \
        "s_sub_u32 s65, s65, 8\n\t"                                               \
        "s_cmp_gt_i32 s65, 20\n\t"                                            \
        "s_cbranch_scc1 .Lloop" #L "_%=\n\t"                                      \
        "s_setprio 1\n\t"                                                         \
        "s_cmp_gt_i32 s65, 8\n\t"                                                 \
        "s_cbranch_scc1 .Lloop" #L "_%=\n\t"                                      \
        "s_setprio 0\n\t"                                                         \
          \
        "s_waitcnt lgkmcnt(0)\n\t"                                                \
        K2_BODY(L##t, MATH, K2_NO, "vmcnt(4)", "vmcnt(2) lgkmcnt(0)", "vmcnt(0)", "vmcnt(0)") \
        "s_branch .Lflush_%=\n"                                                   \
        K2_SW(L##0) K2_SW(L##1) K2_SW(L##2) K2_SW(L##3) K2_SW(L##t0) K2_SW(L##t1) K2_SW(L##t2) K2_SW(L##t3)
#define K2_SW(K) ".Lsw" #K "_%=:\n\ts_swappc_b64 s[62:63], s[60:61]\n\ts_branch .Lrt" #K "_%=\n"
#define K2_ADDA "v_pk_add_f32 v[46:47], v[46:47], v[50:51]\n\t"
#define K2_ADDB "v_pk_add_f32 v[48:49], v[48:49], v[50:51]\n\t"
#define K2_EA(O) "ds_read_b128 v[38:41], v53 offset:" #O "\n\t"
#define K2_EB(O) "ds_read_b128 v[42:45], v53 offset:" #O "\n\t"
#define K2_AA(O) "ds_read_b64 v[46:47], v52 offset:" #O "\n\t"
#define K2_AB(O) "ds_read_b64 v[48:49], v52 offset:" #O "\n\t"
#define K2_NEXT_T2                                                                \
        "v_and_b32 v57, 0xff, v54\n\t"                                            \
        "v_lshrrev_b32 v54, 8, v54\n\t"                                           \
        "v_lshl_add_u32 v58, v57, 8, v50\n\t"                                     \
        "v_lshl_add_u32 v59, v57, 9, v7\n\t"                                      \
        "buffer_load_dwordx4 v[26:29], v58, %[t2r], 0 offen\n\t"
#define K2_CIN  "v_lshlrev_b32 v18, 16, v26\n\tv_lshlrev_b32 v19, 16, v27\n\tv_lshlrev_b32 v20, 16, v28\n\tv_lshlrev_b32 v21, 16, v29\n\t"
#define K2_ACC  "v_mov_b64 v[22:23], v[26:27]\n\tv_mov_b64 v[24:25], v[28:29]\n\tv_mov_b32 v56, v59\n\t"    \
                "v_mov_b64 v[104:105], s[72:73]\n\tv_mov_b64 v[106:107], s[72:73]\n\t"
#define K2_LD(D0, D3, A) "buffer_load_dwordx4 v[" #D0 ":" #D3 "], v" #A ", %[t1r], 0 offen\n\t"
    asm volatile(
        "s_cmp_lg_u32 %[slow], 0\n\t"
        "s_cbranch_scc1 .Ldone_%=\n\t"
        "s_mov_b64 s[74:75], 0\n\t"
        "s_mov_b32 s70, 0\n"
        ".Lstart_%=:\n\t"
        "s_waitcnt lgkmcnt(0)\n\t"
        "ds_read_b128 v[30:33], %[vop]\n\t"
        "ds_read_b128 v[34:37], %[vop] offset:16\n\t"
        "v_mov_b32 v54, %[ordb]\n\t"
        "v_mov_b32 v53, %[vep]\n\t"
        "v_mov_b32 v52, %[vop]\n\t"
        "s_mov_b32 s64, %[lens]\n\t"
        "s_mov_b32 s65, %[tot]\n\t"
        K2_NEXT_T2
        "s_getpc_b64 s[60:61]\n"
        ".Lpc_%=:\n\t"
        "s_add_u32 s60, s60, .Lhnd_%=-.Lpc_%=\n\t"
        "s_addc_u32 s61, s61, 0\n\t"
        "s_and_b32 s66, s64, 0xff\n\t"
        "s_sub_u32 s66, s66, 1\n\t"
        "s_mov_b32 s67, 0\n\t"
        "s_mov_b32 s68, 0xbfb8aa3b\n\t"
        "s_mov_b32 s72, 1.0\n\t"
        "s_mov_b32 s73, 1.0\n\t"
        "s_mov_b32 s69, 0x7b800000\n\t"
        "s_waitcnt lgkmcnt(0)\n\t"
        "v_pk_add_f32 v[30:31], v[30:31], v[50:51]\n\t"
        "v_pk_add_f32 v[32:33], v[32:33], v[50:51]\n\t"
        "v_pk_add_f32 v[34:35], v[34:35], v[50:51]\n\t"
        "v_pk_add_f32 v[36:37], v[36:37], v[50:51]\n\t"
        K2_LD(64, 67, 30) K2_LD(68, 71, 31) K2_LD(72, 75, 32) K2_LD(76, 79, 33)
        "s_cmp_lg_u32 s70, 0\n\t"
        "s_cbranch_scc1 .Lnoe_%=\n\t"
        "s_waitcnt vmcnt(5)\n\t"
        "s_mov_b64 exec, %[m0]\n\tds_write_b64 %[q0], v[108:109] offset:%c[evo]\n\t"
        "s_mov_b64 exec, %[m1]\n\tds_write_b64 %[q1], v[110:111] offset:%c[evo]\n\t"
        "s_mov_b64 exec, %[m2]\n\tds_write_b64 %[q2], v[112:113] offset:%c[evo]\n\t"
        "s_mov_b64 exec, %[m3]\n\tds_write_b64 %[q3], v[114:115] offset:%c[evo]\n\t"
        "s_mov_b64 exec, -1\n\t"
        "s_waitcnt lgkmcnt(0)\n\t"
        "s_barrier\n"
        ".Lnoe_%=:\n\t"
        K2_EA(0) K2_EB(16) K2_AA(32) K2_AB(40)
        "s_setprio 2\n\t"
        "s_waitcnt vmcnt(4) lgkmcnt(3)\n\t"
        K2_CIN K2_ACC
        "v_mfma_f32_16x16x16_bf16 v[96:99], v[38:39], v[14:15], v[18:21]\n\t"
        "v_mfma_f32_16x16x16_bf16 v[100:103], v[40:41], v[14:15], v[18:21]\n\t"
        K2_EA(32)
        "s_waitcnt vmcnt(2)\n\t"
        K2_LD(80, 83, 34) K2_LD(84, 87, 35) K2_LD(88, 91, 36) K2_LD(92, 95, 37)
        "v_mfma_f32_16x16x32_bf16 v[30:33], v[8:11], v[64:67], v[96:99]\n\t"
        "v_mfma_f32_16x16x32_bf16 v[34:37], v[8:11], v[68:71], v[100:103]\n\t"
        "s_mov_b32 s71, 1\n\t"
        "s_cmp_ge_u32 s66, 7\n\t"
        "s_cbranch_scc1 .Lt2l_%=\n\t"
        "s_mov_b32 s71, 0\n\t"
        K2_NEXT_T2
        ".Lt2l_%=:\n\t"
        "s_cmp_lg_u64 s[74:75], 0\n\t"
        "s_nop 2\n\t"
        "s_cbranch_scc1 .Lloopsf_%=\n"
        K2_LOOP(fa, K2_FAST)
        ".Lhnd_%=:\n\t"
        "s_cmp_lg_u32 s67, 0\n\t"
        "s_cbranch_scc1 .Lflush_%=\n\t"
        K2_CIN
        "s_mov_b32 s66, 0\n\t"
        "s_mov_b32 s67, 1\n\t"
        "s_setpc_b64 s[62:63]\n"
        ".Lflush_%=:\n\t"
        "v_pk_fma_f32 v[60:61], v[22:23], 0, v[104:105] op_sel_hi:[1,0,1]\n\t"
        "v_pk_fma_f32 v[62:63], v[24:25], 0, v[106:107] op_sel_hi:[1,0,1]\n\t"
        K2_RCP4(104, 105, 106, 107)
        "v_pk_add_f32 v[60:61], v[60:61], v[62:63]\n\t"
        "v_pk_mul_f32 v[22:23], v[22:23], v[104:105]\n\t"
        "v_add_f32 v60, v60, v61\n\t"
        "v_pk_mul_f32 v[24:25], v[24:25], v[106:107]\n\t"
        "v_cmp_nge_f32 vcc, s69, v60\n\t"
        "s_andn2_b64 vcc, vcc, s[74:75]\n\t"
        "s_cbranch_scc1 .Lredo_%=\n\t"
        "v_pk_mul_f32 v[60:61], v[22:23], s[68:69] op_sel_hi:[1,0]\n\t"
        "v_pk_mul_f32 v[62:63], v[24:25], s[68:69] op_sel_hi:[1,0]\n\t"
        "v_exp_f32 v60, v60\n\t"
        "v_exp_f32 v61, v61\n\t"
        "v_exp_f32 v62, v62\n\t"
        "v_exp_f32 v63, v63\n\t"
        "v_pk_add_f32 v[60:61], v[60:61], 1.0 op_sel_hi:[1,0]\n\t"
        "v_pk_add_f32 v[62:63], v[62:63], 1.0 op_sel_hi:[1,0]\n\t"
        "v_rcp_f32 v60, v60\n\t"
        "v_rcp_f32 v61, v61\n\t"
        "v_rcp_f32 v62, v62\n\t"
        "v_rcp_f32 v63, v63\n\t"
        "v_pk_mul_f32 v[60:61], v[60:61], v[22:23]\n\t"
        "v_pk_mul_f32 v[62:63], v[62:63], v[24:25]\n\t"
        "v_cvt_pk_bf16_f32 v62, v60, v62\n\t"
        "v_cvt_pk_bf16_f32 v63, v61, v63\n\t"
        "s_mov_b32 vcc_lo, 0x55555555\n\t"
        "s_mov_b32 vcc_hi, 0x55555555\n\t"
        "v_cndmask_b32_dpp v60, v63, v62, vcc quad_perm:[1,0,3,2] row_mask:0xf bank_mask:0xf\n\t"
        "s_not_b64 vcc, vcc\n\t"
        "v_cndmask_b32_dpp v61, v62, v63, vcc quad_perm:[1,0,3,2] row_mask:0xf bank_mask:0xf\n\t"
        "s_lshr_b32 s64, s64, 8\n\t"
        "buffer_store_dwordx2 v[60:61], v56, %[outr], 0 offen sc1\n\t"
        "s_and_b32 s66, s64, 0xff\n\t"
        "s_cbranch_scc0 .Ldone_%=\n\t"
        K2_ACC
        K2_NEXT_T2
        "s_sub_u32 s66, s66, 2\n\t"
        "s_mov_b32 s67, 0\n\t"
        "s_setpc_b64 s[62:63]\n"
        ".Legc_code_end:\n"
        K2_LOOP(sf, K2_SAFE)
        ".Lredo_%=:\n\t"
        "s_waitcnt vmcnt(0)\n\t"
        "s_mov_b64 s[74:75], -1\n\t"
        "s_mov_b32 s70, 1\n\t"
        "s_branch .Lstart_%=\n"
        ".Ldone_%=:\n\t"
        "s_waitcnt vmcnt(0) lgkmcnt(0)\n\t"
        :
        : [slow] "s"(slow), [lens] "s"(lens), [tot] "s"(tot), [vop] "v"(vop), [vep] "v"(vep),
          [ordb] "v"(ordb), [evo] "i"(K2_EV), [m0] "s"(vm[0]), [m1] "s"(vm[1]), [m2] "s"(vm[2]), [m3] "s"(vm[3]),
          [q0] "v"(p8[0]), [q1] "v"(p8[1]), [q2] "v"(p8[2]), [q3] "v"(p8[3]), "{v[108:111]}"(re[0]), "{v[112:115]}"(re[1]), [t1r] "s"(t1r), [t2r] "s"(t2r), [outr] "s"(outr), "{v[8:11]}"(asel), "{v[14:15]}"(cfv), "{v[50:51]}"(lo16),
          "{v7}"(olane)
        : "v18", "v19", "v20", "v21", "v22", "v23", "v24", "v25", "v26", "v27", "v28", "v29",
          "v30", "v31", "v32", "v33", "v34", "v35", "v36", "v37", "v38", "v39", "v40", "v41", "v42", "v43", "v44", "v45",
          "v46", "v47", "v48", "v49", "v52", "v53", "v54", "v56", "v57", "v58", "v59", "v60", "v61", "v62", "v63",
          "v64", "v65", "v66", "v67", "v68", "v69", "v70", "v71", "v72", "v73", "v74", "v75", "v76", "v77", "v78", "v79",
          "v80", "v81", "v82", "v83", "v84", "v85", "v86", "v87", "v88", "v89", "v90", "v91", "v92", "v93", "v94", "v95",
          "v96", "v97", "v98", "v99", "v100", "v101", "v102", "v103", "v104", "v105", "v106", "v107",
          "s60", "s61", "s62", "s63", "s64", "s65", "s66", "s67", "s68", "s69", "s70", "s71", "s72", "s73", "s74", "s75",
          "vcc", "scc", "memory");
    if (slow) k2_slow(smem, T1, T2, gw, ea, src, dst, out, N, E, s, node0);
}

}

extern "C" void kernel_launch(void* const* d_in, const int* in_sizes, int n_in,
                              void* d_out, int out_size, void* d_ws, size_t ws_size,
                              hipStream_t stream) {
    const uint16_t* x  = (const uint16_t*)d_in[0];
    const uint16_t* ea = (const uint16_t*)d_in[1];
    const int* src     = (const int*)d_in[2];
    const int* dst     = (const int*)d_in[3];
    const uint16_t* uw = (const uint16_t*)d_in[4];
    const uint16_t* vw = (const uint16_t*)d_in[5];
    const uint16_t* gw = (const uint16_t*)d_in[6];
    const int N = in_sizes[0] / D;
    const int E = in_sizes[2];
    const int nrb   = (N + BM - 1) / BM;
    const int nbins = (N + NBN - 1) / NBN;
    int nchunks = NCH;
    const int slots = nrb + nchunks / 8;
    int ec = (E + NCH * 32 - 1) / (NCH * 32) * 32;
    const int force_slow = (nbins > BINMAX || ec > RMAX * 2048) ? 1 : 0;
    if (force_slow) { nchunks = 0; ec = 0; }

    char* ws = (char*)d_ws;
    const size_t tb = ((size_t)NS * N * FS * 4 + 255) & ~(size_t)255;
    unsigned* T1 = (unsigned*)ws;
    unsigned* T2 = (unsigned*)(ws + tb);
    unsigned* recs = (unsigned*)(ws + 2 * tb);
    unsigned* tab = (unsigned*)(ws + 2 * tb + (((size_t)NCH * ec * 12 + 255) & ~(size_t)255));

    (void)hipFuncSetAttribute((const void*)egc_k1, hipFuncAttributeMaxDynamicSharedMemorySize, K1_LDS);
    egc_k1<<<slots * 8, 512, K1_LDS, stream>>>(x, ea, src, dst, uw, vw, gw, T1, T2, recs, tab,
                                                 N, E, nrb, nbins, ec, nchunks, (N + nrb - 1) / nrb, NBK * 65536 / N);
    egc_k2<<<((nbins + 1) / 2) * 8, 512, 0, stream>>>(T1, T2, recs, tab, gw, ea, src, dst, (uint16_t*)d_out,
                                                      N, E, nchunks, nbins, ec, force_slow);
}
```
